```python
import torch

def _softmax(x, axis=-1):
    return torch.softmax(x, dim=axis)
def _gelu(x):
    return torch.nn.functional.gelu(x, approximate="tanh")
def _silu(x):
    return torch.nn.functional.silu(x)
def _sigmoid(x):
    return torch.sigmoid(x)
def _layernorm(x, axis=-1, eps=1e-5):
    m = x.mean(dim=axis, keepdim=True)
    v = x.var(dim=axis, keepdim=True, unbiased=False)
    return (x - m) / torch.sqrt(v + eps)
def _scatter_add(tgt, idx, src, axis=0):
    return tgt.clone().index_add_(axis, idx, src)
def _segment_sum(src, seg, n):
    out = torch.zeros((n,) + src.shape[1:], dtype=src.dtype)
    return out.index_add_(0, seg, src)
def _pad(x, pw):
    flat = tuple(v for pair in reversed(pw) for v in pair)
    return torch.nn.functional.pad(x, flat)

kernel_name = 'concat_heads__softmax_full_mha__ssm_conv_h3__prompt'

def setup_inputs():

    _w = (lambda a: (lambda t: t.to(torch.bfloat16) if t.is_floating_point() else t)(torch.as_tensor(a)))
    def _A_setup():
        B, S, D = 1, 2048, 576
        return {
            "x": _w((lambda *s: torch.randn(*s, dtype=torch.bfloat16) * 0.2)(B, S, D)),
            "wqkv": _w((lambda *s: torch.randn(*s, dtype=torch.bfloat16) * 0.2)(D, 3 * D)),
            "wo": _w((lambda *s: torch.randn(*s, dtype=torch.bfloat16) * 0.05)(D, D)),
        }
    _A_in = _A_setup()

    def _B_setup():
        B, S, D = 1, 2048, 576
        ks = 4
        return {
            "q": _w((lambda *s: torch.randn(*s, dtype=torch.bfloat16) * 0.05)(B, S, D)),
            "k": _w((lambda *s: torch.randn(*s, dtype=torch.bfloat16) * 0.4)(B, S, D)),
            "v": _w((lambda *s: torch.randn(*s, dtype=torch.bfloat16) * 0.4)(B, S, D)),
            "shift_k": _w((lambda *s: torch.randn(*s, dtype=torch.bfloat16) * 0.4)(D, ks) * 0.5),
            "ssm_k": _w((lambda *s: torch.randn(*s, dtype=torch.bfloat16) * 1.6)(D, S) * (1.0 / S ** 0.5)),
            "ssm_D": _w((lambda *s: torch.randn(*s, dtype=torch.bfloat16) * 0.4)(D)),
        }
    _B_in = _B_setup()

    return {
        "A_x": _A_in["x"],
        "A_wqkv": _A_in["wqkv"],
        "A_wo": _A_in["wo"],
        "B_q": _B_in["q"],
        "B_k": _B_in["k"],
        "B_v": _B_in["v"],
        "B_shift_k": _B_in["shift_k"],
        "B_ssm_k": _B_in["ssm_k"],
        "B_ssm_D": _B_in["ssm_D"],
    }

def reference(A_x, A_wqkv, A_wo, B_q, B_k, B_v, B_shift_k, B_ssm_k, B_ssm_D):
    def _A_ref(x, wqkv, wo):
        B, S, D = x.shape
        H = 32; Dh = D // H
        qkv = (lambda x, *d: x.permute(*d))((x @ wqkv).reshape(B, S, 3, H, Dh), 2, 0, 3, 1, 4)
        q, k, v = qkv[0], qkv[1], qkv[2]
        scale = Dh ** -0.5
        scores = torch.einsum("bhqd,bhkd->bhqk", q, k) * scale
        p = _softmax(scores, axis=-1)
        o = torch.einsum("bhqk,bhkd->bhqd", p, v)
        return (lambda x, *d: x.permute(*d))(o, 0, 2, 1, 3).reshape(B, S, D) @ wo

    def _B_ref(q, k, v, shift_k, ssm_k, ssm_D):
        B, S, D = q.shape
        ks = shift_k.shape[-1]
        k_pad = _pad(k, ((0, 0), (ks - 1, 0), (0, 0)))
        k_shift = torch.zeros_like(k)
        for t in range(ks):
            k_shift = k_shift + k_pad[:, t:t + S, :] * shift_k[:, ks - 1 - t][None, None, :]
        kv = k_shift * v
        L = 2 * S
        kv_pad = _pad(kv, ((0, 0), (0, S), (0, 0)))
        kf_pad = _pad(ssm_k, ((0, 0), (0, S)))
        kv_f = (lambda x, axes=(-2,-1): torch.fft.rfft2(x.float(), dim=axes))(kv_pad[:, None, :, :], axes=(1, 2))
        kf_f = (lambda x, axes=(-2,-1): torch.fft.rfft2(x.float(), dim=axes))(kf_pad[:, None, :], axes=(1, 2))
        kf_f = (lambda x, *d: x.permute(*d))(kf_f, 1, 2, 0)
        y_f = kv_f * kf_f[None, :, :, :]
        y = (lambda x, s, axes=(-2,-1): torch.fft.irfft2(x, s=s, dim=axes).to(torch.bfloat16))(y_f, (1, L), axes=(1, 2))[:, 0, :S, :]
        y = y + kv * ssm_D[None, None, :]
        return q * y

    a = _A_ref(A_x, A_wqkv, A_wo)
    b = _B_ref(B_q, B_k, B_v, B_shift_k, B_ssm_k, B_ssm_D)
    return torch.concatenate([a, b], axis=-1)
```

```cpp
#include <hip/hip_runtime.h>
#include <hip/hip_bf16.h>
#include <stdint.h>

#define LAS __attribute__((address_space(3)))
typedef __attribute__((ext_vector_type(8))) __bf16 bf16x8;
typedef __attribute__((ext_vector_type(16))) __bf16 bf16x16;
typedef __attribute__((ext_vector_type(4))) short s16x4;
typedef __attribute__((ext_vector_type(8))) short s16x8;
typedef __attribute__((ext_vector_type(16))) float f32x16;
typedef __attribute__((ext_vector_type(8))) float f32x8;
typedef __attribute__((ext_vector_type(4))) float f32x4;
typedef __attribute__((ext_vector_type(4))) unsigned u32x4;
typedef __attribute__((ext_vector_type(2))) unsigned u32x2;
typedef unsigned short u16;

constexpr int S = 2048, D = 576, NH = 32;
constexpr int OUTW = 2 * D;
constexpr int KF_B = 65536, K2_B = 8192, VT_B = 128 * 608, HEAD_B = KF_B + K2_B + VT_B;
constexpr int QT_B = 1152, QH_B = 64 * QT_B;
constexpr size_t WS_KV = 0;
constexpr size_t WS_Q = WS_KV + (size_t)NH * HEAD_B;
constexpr size_t WS_O = WS_Q + (size_t)NH * QH_B;
constexpr size_t WS_KVT = WS_O + (size_t)S * D * 2;
constexpr size_t WS_ZT = WS_KVT + (size_t)S * D * 2;
constexpr float QSCALE = 0.23570226039551584f * 1.4426950408889634f;

__device__ __forceinline__ void glds16(const void* g, unsigned lds_base) {
    unsigned sv;
    asm volatile("s_mov_b32 %0, m0\n\ts_mov_b32 m0, %2\n\ts_nop 0\n\tglobal_load_lds_dwordx4 %1, off\n\ts_mov_b32 m0, %0"
                 : "=&s"(sv) : "v"(g), "s"(lds_base) : "memory");
}
__device__ __forceinline__ unsigned cvtpk(float lo, float hi) {
    typedef __attribute__((ext_vector_type(2))) float f2; typedef __attribute__((ext_vector_type(2))) __bf16 b2;
    f2 v = {lo, hi};
    return __builtin_bit_cast(unsigned, __builtin_convertvector(v, b2));
}
__device__ __forceinline__ float bflo(unsigned u) { return __uint_as_float(u << 16); }
__device__ __forceinline__ float bfhi(unsigned u) { return __uint_as_float(u & 0xffff0000u); }
__device__ __forceinline__ float rbf(float x) { return bflo(cvtpk(x, 0.f)); }
__device__ __forceinline__ s16x4 trread(unsigned lds_addr) {
    return __builtin_bit_cast(s16x4, __builtin_amdgcn_ds_read_tr16_b64_v4i16((LAS s16x4*)(uintptr_t)lds_addr));
}
__device__ __forceinline__ bf16x8 cat8(s16x4 a, s16x4 b) {
    s16x8 r = __builtin_shufflevector(a, b, 0, 1, 2, 3, 4, 5, 6, 7);
    return __builtin_bit_cast(bf16x8, r);
}
__device__ __forceinline__ bf16x8 ldsrd128(unsigned lds_addr) { return *(LAS const bf16x8*)(uintptr_t)lds_addr; }
#define MFMA32(a, b, c) __builtin_amdgcn_mfma_f32_32x32x16_bf16(a, b, c, 0, 0, 0)

constexpr int K1_STAGE = 32768, K1_TR = 4 * K1_STAGE, K1_LDS = K1_TR + 64 * 144;

__device__ __forceinline__ void kv_tile_compute(const u32x4& k0, const u32x4& k1, const u32x4& k2, const u32x4& k3, const u32x4& vv,
                                                const u16* __restrict__ shift_k, int c, int trow, unsigned tr_lds, int chunk) {
    unsigned r[4];
#pragma unroll
    for (int j = 0; j < 4; ++j) {
        const u32x2 wa = *(const u32x2*)(shift_k + (c + 2 * j) * 4), wb = *(const u32x2*)(shift_k + (c + 2 * j + 1) * 4);
        float a, b;
        {   unsigned p0 = cvtpk(bflo(k0[j]) * bfhi(wa[1]), bfhi(k0[j]) * bfhi(wb[1]));
            unsigned p1 = cvtpk(bflo(k1[j]) * bflo(wa[1]), bfhi(k1[j]) * bflo(wb[1]));
            unsigned s = cvtpk(bflo(p0) + bflo(p1), bfhi(p0) + bfhi(p1));
            unsigned p2 = cvtpk(bflo(k2[j]) * bfhi(wa[0]), bfhi(k2[j]) * bfhi(wb[0]));
            s = cvtpk(bflo(s) + bflo(p2), bfhi(s) + bfhi(p2));
            unsigned p3 = cvtpk(bflo(k3[j]) * bflo(wa[0]), bfhi(k3[j]) * bflo(wb[0]));
            s = cvtpk(bflo(s) + bflo(p3), bfhi(s) + bfhi(p3));
            a = bflo(s) * bflo(vv[j]); b = bfhi(s) * bfhi(vv[j]); }
        r[j] = cvtpk(a, b);
    }
    const int row = (trow & ~7) | (7 - (trow & 7));
    *(LAS u32x4*)(uintptr_t)(tr_lds + row * 144 + chunk * 16) = (u32x4){r[0], r[1], r[2], r[3]};
}

__device__ __forceinline__ void kv_tile_loads(const u16* __restrict__ bk, const u16* __restrict__ bv, int t, int c,
                                              u32x4& k0, u32x4& k1, u32x4& k2, u32x4& k3, u32x4& vv) {
    const u32x4 z = {0u, 0u, 0u, 0u};
    const u16* kp = bk + (size_t)t * D + c;
    k3 = *(const u32x4*)kp;
    k2 = t >= 1 ? *(const u32x4*)(kp - D) : z;
    k1 = t >= 2 ? *(const u32x4*)(kp - 2 * D) : z;
    k0 = t >= 3 ? *(const u32x4*)(kp - 3 * D) : z;
    vv = *(const u32x4*)(bv + (size_t)t * D + c);
}
__device__ __forceinline__ void kv_tile_store(u16* __restrict__ kvt, int t0, int c0, unsigned tr_lds, int w, int lane) {
    const int G = lane >> 4, q = (lane & 15) >> 2, p = lane & 3, beta = (w >> 2) * 4 + G;
    const unsigned a = tr_lds + (8 * beta + q) * 144 + (16 * (w & 3) + 4 * p) * 2;
    const s16x4 lo = trread(a), hi = trread(a + 4 * 144);
    const s16x8 r = __builtin_shufflevector(lo, hi, 0, 1, 2, 3, 4, 5, 6, 7);
    *(s16x8*)(kvt + (size_t)(c0 + 16 * (w & 3) + (lane & 15)) * S + t0 + 8 * beta) = r;
}
__device__ __forceinline__ void kv_tile(const u16* bk, const u16* bv, const u16* shift_k, u16* kvt, int tile, unsigned tr_lds) {
    const int tid = threadIdx.x, lane = tid & 63, w = __builtin_amdgcn_readfirstlane(tid >> 6);
    const int t0 = (tile / 9) * 64, c0 = (tile % 9) * 64;
    const int trow = tid >> 3, chunk = tid & 7;
    u32x4 k0, k1, k2, k3, vv;
    kv_tile_loads(bk, bv, t0 + trow, c0 + chunk * 8, k0, k1, k2, k3, vv);
    kv_tile_compute(k0, k1, k2, k3, vv, shift_k, c0 + chunk * 8, trow, tr_lds, chunk);
    __syncthreads();
    kv_tile_store(kvt, t0, c0, tr_lds, w, lane);
}

template <bool ISV>
__device__ __forceinline__ void qkv_gemm(const u16* __restrict__ x, const u16* __restrict__ wqkv, char* __restrict__ ws,
                                         int mb, int ng, unsigned lds0, int w, int lane) {
    const int ctl = w & 3, tb0 = (w >> 2) * 2;
    const bool active = (ng * 4 + ctl) < 54;
    const u16* xsrc[2]; const u16* wsrc[2];
#pragma unroll
    for (int i = 0; i < 2; ++i) {
        const int p = w * 2 + i;
        const int xr = 8 * p + (lane >> 3), xc = (lane & 7) ^ ((xr >> 1) & 7);
        xsrc[i] = x + (size_t)(mb * 128 + xr) * D + xc * 8;
        const int wr = 4 * p + (lane >> 4), wc = (lane & 15) ^ (((wr & 3) << 2) | ((wr >> 2) & 3));
        int col = ng * 128 + wc * 8; col = col > 1720 ? 1720 : col;
        wsrc[i] = wqkv + (size_t)wr * (3 * D) + col;
    }
    auto issue = [&](int c) {
        const unsigned st = lds0 + (c & 3) * K1_STAGE;
#pragma unroll
        for (int i = 0; i < 2; ++i) {
            glds16(xsrc[i] + c * 64, st + (w * 2 + i) * 1024);
            glds16(wsrc[i] + (size_t)c * 64 * (3 * D), st + 16384 + (w * 2 + i) * 1024);
        }
    };
    const int r = lane & 31, h = lane >> 5;
    unsigned xa[4];
#pragma unroll
    for (int s = 0; s < 4; ++s) xa[s] = (tb0 * 32 + r) * 128 + (((2 * s + h) ^ ((r >> 1) & 7)) << 4);
    unsigned wa[2];
    {   const int blk = (lane >> 4) & 1, q = (lane & 15) >> 2, p = lane & 3;
#pragma unroll
        for (int u = 0; u < 2; ++u) {
            const int row = 8 * h + 4 * u + q, ch = 4 * ctl + 2 * blk + (p >> 1);
            wa[u] = 16384 + row * 256 + ((ch ^ (((row & 3) << 2) | ((row >> 2) & 3))) << 4) + 8 * (p & 1);
        } }
    f32x16 acc0 = {}, acc1 = {};
    issue(0); issue(1); issue(2);
#pragma unroll 1
    for (int c = 0; c < 9; ++c) {
        if (c < 7) asm volatile("s_waitcnt vmcnt(8)" ::: "memory");
        else if (c == 7) asm volatile("s_waitcnt vmcnt(4)" ::: "memory");
        else asm volatile("s_waitcnt vmcnt(0)" ::: "memory");
        asm volatile("s_waitcnt lgkmcnt(0)\n\ts_barrier" ::: "memory");
        if (c + 3 < 9) issue(c + 3);
        if (active) {
            const unsigned st = lds0 + (c & 3) * K1_STAGE;
#pragma unroll
            for (int s = 0; s < 4; ++s) {
                const bf16x8 wf = cat8(trread(st + wa[0] + s * 4096), trread(st + wa[1] + s * 4096));
                const bf16x8 x0 = ldsrd128(st + xa[s]), x1 = ldsrd128(st + xa[s] + 4096);
                if (ISV) { acc0 = MFMA32(x0, wf, acc0); acc1 = MFMA32(x1, wf, acc1); }
                else     { acc0 = MFMA32(wf, x0, acc0); acc1 = MFMA32(wf, x1, acc1); }
            }
        }
    }
    if (!active) return;
    const int ct = ng * 4 + ctl;
#pragma unroll
    for (int i = 0; i < 2; ++i) {
        const f32x16& acc = i ? acc1 : acc0;
        const int tile = mb * 4 + tb0 + i;
        if (ISV) {
            const int col = (ct - 36) * 32 + r, head = col / 18, d = col - head * 18;
            char* vp = ws + WS_KV + (size_t)head * HEAD_B + KF_B + K2_B + (2 * tile) * 608 + h * 304 + d * 16;
#pragma unroll
            for (int g = 0; g < 2; ++g) {
                const u32x4 o = {cvtpk(acc[8 * g], acc[8 * g + 1]), cvtpk(acc[8 * g + 2], acc[8 * g + 3]),
                                 cvtpk(acc[8 * g + 4], acc[8 * g + 5]), cvtpk(acc[8 * g + 6], acc[8 * g + 7])};
                *(u32x4*)(vp + g * 608) = o;
                if (d == 17) *(u32x4*)(vp + g * 608 + 16) = (u32x4){0x3f803f80u, 0x3f803f80u, 0x3f803f80u, 0x3f803f80u};
            }
        } else {
            const bool isq = ct < 18;
            const float sc = isq ? QSCALE : 1.0f;
            const int ctp = isq ? ct : ct - 18;
            const int row = isq ? r : ((r & 16) | ((r & 4) << 1) | ((r & 8) >> 1) | (r & 3));
            char* mainb = isq ? ws + WS_Q + (size_t)tile * QT_B : ws + WS_KV + (size_t)tile * 1024;
            char* remb = isq ? ws + WS_Q + (size_t)tile * QT_B + 1024 : ws + WS_KV + KF_B + (size_t)tile * 128;
            const size_t hstride = isq ? (size_t)QH_B : (size_t)HEAD_B;
#pragma unroll
            for (int g = 0; g < 4; ++g) {
                const int qd = 8 * ctp + 2 * g + h, pr = qd / 9, pos = qd - 9 * pr;
                const unsigned lo = cvtpk(acc[4 * g] * sc, acc[4 * g + 1] * sc), hi = cvtpk(acc[4 * g + 2] * sc, acc[4 * g + 3] * sc);
                if (pos == 4) {
                    *(unsigned*)(remb + (size_t)(2 * pr) * hstride + row * 4) = lo;
                    *(unsigned*)(remb + (size_t)(2 * pr + 1) * hstride + row * 4) = hi;
                } else {
                    const int head = 2 * pr + (pos > 4), u = pos > 4 ? pos - 5 : pos;
                    *(u32x2*)(mainb + (size_t)head * hstride + (u >> 1) * 512 + row * 16 + (u & 1) * 8) = (u32x2){lo, hi};
                }
            }
        }
    }
}

__global__ __launch_bounds__(512, 2) void k1_qkv(const u16* __restrict__ x, const u16* __restrict__ wqkv, const u16* __restrict__ bk,
                                                 const u16* __restrict__ bv, const u16* __restrict__ shift_k, char* __restrict__ ws) {
    extern __shared__ __attribute__((aligned(16))) char lds[];
    const unsigned lds0 = (unsigned)(uintptr_t)lds;
    const int b = blockIdx.x, tid = threadIdx.x, lane = tid & 63, w = __builtin_amdgcn_readfirstlane(tid >> 6);
    u16* kvt = (u16*)(ws + WS_KVT);
    if (b >= 224) {
        const int t = 224 + (b - 224) * 2;
        kv_tile(bk, bv, shift_k, kvt, t, lds0 + K1_TR);
        __syncthreads();
        kv_tile(bk, bv, shift_k, kvt, t + 1, lds0 + K1_TR);
        return;
    }
    kv_tile(bk, bv, shift_k, kvt, b, lds0 + K1_TR);
    const int xcd = b & 7, i = b >> 3, mb = (xcd >> 1) * 4 + (i & 3), ng = (xcd & 1) * 7 + (i >> 2);
    if (ng >= 9) qkv_gemm<true>(x, wqkv, ws, mb, ng, lds0, w, lane);
    else qkv_gemm<false>(x, wqkv, ws, mb, ng, lds0, w, lane);
}

constexpr int K2_LDS = HEAD_B + 4096;

__device__ __forceinline__ void attn_slow(unsigned ka, unsigned k2a, unsigned va, bf16x8 qf, s16x4 q2, int ntile, f32x16& o, float& mrow) {
    float m = -3.0e38f;
    o = f32x16{};
    for (int t = 0; t < ntile; ++t) {
        const bf16x8 kf = ldsrd128(ka + t * 1024);
        const unsigned k2 = *(LAS const unsigned*)(uintptr_t)(k2a + t * 128);
        f32x16 s = {};
        s = MFMA32(kf, qf, s);
        s = __builtin_amdgcn_mfma_f32_32x32x8bf16_1k(__builtin_bit_cast(s16x4, (u32x2){k2, 0u}), q2, s, 0, 0, 0);
        float tm = s[0];
#pragma unroll
        for (int r = 1; r < 16; ++r) tm = fmaxf(tm, s[r]);
        tm = fmaxf(tm, __shfl_xor(tm, 32));
        const float mn = fmaxf(m, tm);
        const float f = __builtin_amdgcn_exp2f(m - mn);
        m = mn;
#pragma unroll
        for (int r = 0; r < 16; ++r) { o[r] *= f; s[r] = __builtin_amdgcn_exp2f(s[r] - mn); }
        const f32x8 lo = __builtin_shufflevector(s, s, 0, 1, 2, 3, 4, 5, 6, 7), hi = __builtin_shufflevector(s, s, 8, 9, 10, 11, 12, 13, 14, 15);
        o = __builtin_amdgcn_smfmac_f32_32x32x32_bf16(ldsrd128(va + t * 1216), __builtin_bit_cast(bf16x16, lo), o, (int)0xDDDDDDDD, 0, 0);
        o = __builtin_amdgcn_smfmac_f32_32x32x32_bf16(ldsrd128(va + t * 1216 + 608), __builtin_bit_cast(bf16x16, hi), o, (int)0xDDDDDDDD, 0, 0);
    }
    mrow = m;
}

__device__ __forceinline__ bool attn_range_ok(const f32x16& o) {
    const float l = o[10];
    bool ok = (l >= 5.9604645e-8f) && (l <= 1.8446744e19f);
#pragma unroll
    for (int r = 0; r < 10; ++r) ok = ok && (fabsf(o[r]) <= 3.0e38f);
    return __all(ok);
}

__global__ __launch_bounds__(512, 2) void k2_attn(char* __restrict__ ws) {
    extern __shared__ __attribute__((aligned(16))) char lds[];
    const unsigned lds0 = (unsigned)(uintptr_t)lds;
    const int tid = threadIdx.x, lane = tid & 63, w = __builtin_amdgcn_readfirstlane(tid >> 6);
    const int b = blockIdx.x, head = (b & 7) * 4 + ((b >> 3) & 3), qb = b >> 5;
    const char* src = ws + WS_KV + (size_t)head * HEAD_B + lane * 16;
    for (int p = w; p < 148; p += 8) glds16(src + p * 1024, lds0 + p * 1024);
    const int qp = w & 3, kpart = w >> 2;
    const int qtA = qb * 8 + qp * 2;
    const char* qsrc = ws + WS_Q + (size_t)head * QH_B + (size_t)qtA * QT_B;
    bf16x8 qfA = *(const bf16x8*)(qsrc + lane * 16), qfB = *(const bf16x8*)(qsrc + QT_B + lane * 16);
    const unsigned q2Ar = *(const unsigned*)(qsrc + 1024 + (lane & 31) * 4), q2Br = *(const unsigned*)(qsrc + QT_B + 1024 + (lane & 31) * 4);
    s16x4 q2A = __builtin_bit_cast(s16x4, (u32x2){lane < 32 ? q2Ar : 0u, 0u}), q2B = __builtin_bit_cast(s16x4, (u32x2){lane < 32 ? q2Br : 0u, 0u});
    const int m = lane & 31, h = lane >> 5;
    const int vrow = (m < 19) ? m : ((m == 22) ? 18 : 0);
    const unsigned ka0 = lds0 + lane * 16 + kpart * 32 * 1024;
    const unsigned k2a0 = lds0 + KF_B + m * 4 + kpart * 32 * 128;
    const unsigned va0 = lds0 + KF_B + K2_B + h * 304 + vrow * 16 + kpart * 32 * 1216;
    asm volatile("s_waitcnt vmcnt(0)\n\ts_barrier" ::: "memory");

    f32x16 oA = {}, oB = {};
    {
        unsigned ka = ka0, k2a = k2a0, va = va0, idx = 0xDDDDDDDDu;
        int cnt = 16;
#define EXP4(d0, d1, d2, d3, s0, s1, s2, s3) \
        "v_exp_f32 v" #d0 ", v" #s0 "\n v_exp_f32 v" #d1 ", v" #s1 "\n v_exp_f32 v" #d2 ", v" #s2 "\n v_exp_f32 v" #d3 ", v" #s3 "\n"
#define KEYTILE(OK, OK2, OVH, OVL) \
        "v_mfma_f32_32x32x16_bf16 v[80:95], v[112:115], %3, 0\n" \
        "ds_read_b128 v[112:115], %7 offset:" #OK "\n" \
        EXP4(96, 97, 98, 99, 64, 65, 66, 67) \
        "v_mfma_f32_32x32x8_bf16 v[80:95], v[116:117], %5, v[80:95]\n" \
        "ds_read_b32 v116, %8 offset:" #OK2 "\n" \
        EXP4(100, 101, 102, 103, 68, 69, 70, 71) \
        "v_smfmac_f32_32x32x32_bf16 %1, v[122:125], v[104:111], %6\n" \
        "ds_read_b128 v[122:125], %9 offset:" #OVH "\n" \
        EXP4(104, 105, 106, 107, 72, 73, 74, 75) \
        "s_waitcnt lgkmcnt(3)\n" \
        "v_smfmac_f32_32x32x32_bf16 %0, v[118:121], v[96:103], %6\n" \
        EXP4(108, 109, 110, 111, 76, 77, 78, 79) \
        "s_waitcnt lgkmcnt(2)\n" \
        "v_mfma_f32_32x32x16_bf16 v[64:79], v[112:115], %2, 0\n" \
        EXP4(96, 97, 98, 99, 80, 81, 82, 83) \
        "s_waitcnt lgkmcnt(1)\n" \
        "v_mfma_f32_32x32x8_bf16 v[64:79], v[116:117], %4, v[64:79]\n" \
        EXP4(100, 101, 102, 103, 84, 85, 86, 87) \
        "s_waitcnt lgkmcnt(0)\n" \
        "v_smfmac_f32_32x32x32_bf16 %0, v[122:125], v[104:111], %6\n" \
        EXP4(104, 105, 106, 107, 88, 89, 90, 91) \
        "v_smfmac_f32_32x32x32_bf16 %1, v[118:121], v[96:103], %6\n" \
        "ds_read_b128 v[118:121], %9 offset:" #OVL "\n" \
        EXP4(108, 109, 110, 111, 92, 93, 94, 95)
        asm volatile(
            "ds_read_b128 v[112:115], %7\n"
            "ds_read_b32 v116, %8\n"
            "v_mov_b32 v117, 0\n"
            "v_mov_b32 v104, 0\n v_mov_b32 v105, 0\n v_mov_b32 v106, 0\n v_mov_b32 v107, 0\n"
            "v_mov_b32 v108, 0\n v_mov_b32 v109, 0\n v_mov_b32 v110, 0\n v_mov_b32 v111, 0\n"
            "v_mov_b32 v122, 0\n v_mov_b32 v123, 0\n v_mov_b32 v124, 0\n v_mov_b32 v125, 0\n"
            "s_waitcnt lgkmcnt(0)\n"
            "v_mfma_f32_32x32x16_bf16 v[64:79], v[112:115], %2, 0\n"
            "v_mfma_f32_32x32x8_bf16 v[64:79], v[116:117], %4, v[64:79]\n"
            "ds_read_b128 v[118:121], %9\n"
            "s_nop 7\n s_nop 7\n"
            "1:\n"
            KEYTILE(1024, 128, 608, 1216)
            KEYTILE(2048, 256, 1824, 2432)
            "v_add_u32 %7, 2048, %7\n v_add_u32 %8, 256, %8\n v_add_u32 %9, 2432, %9\n"
            "s_sub_u32 %10, %10, 1\n s_cmp_lg_u32 %10, 0\n s_cbranch_scc1 1b\n"
            "s_waitcnt lgkmcnt(0)\n"
            "v_smfmac_f32_32x32x32_bf16 %1, v[122:125], v[104:111], %6\n"
            "s_nop 7\n s_nop 7\n"
            : "+v"(oA), "+v"(oB), "+v"(qfA), "+v"(qfB), "+v"(q2A), "+v"(q2B), "+v"(idx), "+v"(ka), "+v"(k2a), "+v"(va), "+s"(cnt)
            :
            : "v64", "v65", "v66", "v67", "v68", "v69", "v70", "v71", "v72", "v73", "v74", "v75", "v76", "v77", "v78", "v79",
              "v80", "v81", "v82", "v83", "v84", "v85", "v86", "v87", "v88", "v89", "v90", "v91", "v92", "v93", "v94", "v95",
              "v96", "v97", "v98", "v99", "v100", "v101", "v102", "v103", "v104", "v105", "v106", "v107", "v108", "v109", "v110", "v111",
              "v112", "v113", "v114", "v115", "v116", "v117", "v118", "v119", "v120", "v121", "v122", "v123", "v124", "v125", "scc", "memory");
#undef KEYTILE
#undef EXP4
    }
    float mA = 0.f, mB = 0.f;
    if (!attn_range_ok(oA)) attn_slow(ka0, k2a0, va0, qfA, q2A, 32, oA, mA);
    if (!attn_range_ok(oB)) attn_slow(ka0, k2a0, va0, qfB, q2B, 32, oB, mB);
    __syncthreads();
    {
        float* xch = (float*)lds + (size_t)w * 12 * 64;
        const f32x16& give = kpart ? oA : oB;
#pragma unroll
        for (int r = 0; r < 11; ++r) xch[r * 64 + lane] = give[r];
        xch[11 * 64 + lane] = kpart ? mA : mB;
    }
    __syncthreads();
    {
        const float* xin = (const float*)lds + (size_t)(w ^ 4) * 12 * 64;
        const f32x16& keep = kpart ? oB : oA;
        const float m0 = kpart ? mB : mA, m1 = xin[11 * 64 + lane];
        const float mm = fmaxf(m0, m1), f0 = __builtin_amdgcn_exp2f(m0 - mm), f1 = __builtin_amdgcn_exp2f(m1 - mm);
        float v[11];
#pragma unroll
        for (int r = 0; r < 11; ++r) v[r] = keep[r] * f0 + xin[r * 64 + lane] * f1;
        const float inv = 1.0f / v[10];
        const int tb = qtA + kpart;
        char* op = ws + WS_O + (size_t)tb * 36 * 1024;
        const u32x4 o8 = {cvtpk(v[0] * inv, v[1] * inv), cvtpk(v[2] * inv, v[3] * inv), cvtpk(v[4] * inv, v[5] * inv), cvtpk(v[6] * inv, v[7] * inv)};
        *(u32x4*)(op + head * 1024 + lane * 16) = o8;
        if (h == 0) *(unsigned*)(op + (32 + (head >> 3)) * 1024 + (((head & 7) >> 2) * 32 + m) * 16 + (head & 3) * 4) = cvtpk(v[8] * inv, v[9] * inv);
    }
}

constexpr int CV_IMG = 0, CV_IMG_B = (32 + 2048) * 16, CV_U = CV_IMG_B, CV_U_B = 95 * 80, CV_LDS = CV_U + CV_U_B;

__global__ __launch_bounds__(256, 2) void k_conv(const u16* __restrict__ ssm_k, const u16* __restrict__ ssm_D, char* __restrict__ ws) {
    extern __shared__ __attribute__((aligned(16))) char lds[];
    const unsigned lds0 = (unsigned)(uintptr_t)lds;
    const int c = blockIdx.x, tid = threadIdx.x, lane = tid & 63, w = __builtin_amdgcn_readfirstlane(tid >> 6);
    const u16* hrow = ssm_k + (size_t)c * S;
#pragma unroll
    for (int i = 0; i < 8; ++i) {
        const int p = w * 8 + i; int x = 64 * p + lane; x = x > 2040 ? 2040 : x;
        glds16(hrow + x, lds0 + CV_IMG + 512 + p * 1024);
    }
    const u32x4 uu = *(const u32x4*)((const u16*)(ws + WS_KVT) + (size_t)c * S + tid * 8);
    *(LAS u32x4*)(uintptr_t)(lds0 + CV_U + (31 + (tid >> 2)) * 80 + (tid & 3) * 16) = uu;
    if (tid < 155) *(LAS u32x4*)(uintptr_t)(lds0 + CV_U + tid * 16) = (u32x4){0u, 0u, 0u, 0u};
    if (tid < 32) {
        const int x = tid - 32; unsigned e[8];
#pragma unroll
        for (int j = 0; j < 8; ++j) e[j] = (x + j >= 0) ? hrow[x + j] : 0;
        *(LAS u32x4*)(uintptr_t)(lds0 + CV_IMG + tid * 16) = (u32x4){e[0] | (e[1] << 16), e[2] | (e[3] << 16), e[4] | (e[5] << 16), e[6] | (e[7] << 16)};
    }
    asm volatile("s_waitcnt vmcnt(0) lgkmcnt(0)\n\ts_barrier" ::: "memory");
    const int n = lane & 31, hh = lane >> 5;
    const unsigned aaddr = lds0 + CV_IMG + (n - 8 * hh + 9) * 16;
    const unsigned baddr = lds0 + CV_U + (n + 31) * 80 + hh * 16;
    const int jb = w == 0 ? 0 : w == 1 ? 24 : w == 2 ? 48 : 80, je = w == 0 ? 24 : w == 1 ? 48 : w == 2 ? 80 : 128;
    f32x16 d0 = {}, d1 = {};
    for (int j = jb; j < je; ++j) {
        const bf16x8 a = ldsrd128(aaddr + j * 256);
        const int c2 = 1040 - 16 * j;
        const int o1 = (c2 >> 5) * 80 + (c2 & 16) * 2;
        d1 = MFMA32(a, ldsrd128(baddr + o1), d1);
        if (j < 64) d0 = MFMA32(a, ldsrd128(baddr + o1 - 32 * 80), d0);
    }
    __syncthreads();
    float* part = (float*)lds + w * 2048;
#pragma unroll
    for (int g = 0; g < 4; ++g) {
        const int t = 32 * n + 8 * g + 4 * hh;
        *(f32x4*)(part + t) = (f32x4){d0[4 * g], d0[4 * g + 1], d0[4 * g + 2], d0[4 * g + 3]};
        *(f32x4*)(part + 1024 + t) = (f32x4){d1[4 * g], d1[4 * g + 1], d1[4 * g + 2], d1[4 * g + 3]};
    }
    __syncthreads();
    {
        const float* p = (const float*)lds + tid * 8;
        float y[8];
#pragma unroll
        for (int j = 0; j < 8; ++j) y[j] = p[j] + p[2048 + j] + p[4096 + j] + p[6144 + j];
        const float dd = bflo(ssm_D[c]);
        unsigned o[4];
#pragma unroll
        for (int j = 0; j < 4; ++j) {
            const float u0 = bfhi(uu[3 - j]), u1 = bflo(uu[3 - j]);
            const float z0 = rbf(y[2 * j]) + rbf(u0 * dd), z1 = rbf(y[2 * j + 1]) + rbf(u1 * dd);
            o[j] = cvtpk(z0, z1);
        }
        *(u32x4*)((u16*)(ws + WS_ZT) + (size_t)c * S + tid * 8) = (u32x4){o[0], o[1], o[2], o[3]};
    }
}

constexpr int K3_LDS = 576 * 128;

__global__ __launch_bounds__(256, 2) void k3_out(const u16* __restrict__ wo, const u16* __restrict__ bq, const char* __restrict__ ws, u16* __restrict__ out) {
    extern __shared__ __attribute__((aligned(16))) char lds[];
    const unsigned lds0 = (unsigned)(uintptr_t)lds;
    const int b = blockIdx.x, tid = threadIdx.x, lane = tid & 63, w = __builtin_amdgcn_readfirstlane(tid >> 6);
    if (b >= 198) {
        const int tile = b - 198, t0 = (tile / 9) * 64, c0 = (tile % 9) * 64;
        const u16* zt = (const u16*)(ws + WS_ZT);
#pragma unroll
        for (int i = 0; i < 2; ++i) {
            const int it = tid + 256 * i, ch = it >> 3, k = it & 7;
            *(LAS u32x4*)(uintptr_t)(lds0 + ch * 144 + k * 16) = *(const u32x4*)(zt + (size_t)(c0 + ch) * S + t0 + k * 8);
        }
        __syncthreads();
#pragma unroll
        for (int i = 0; i < 2; ++i) {
            const int G = lane >> 4, q = (lane & 15) >> 2, p = lane & 3, cb = G + 4 * i;
            const unsigned a = lds0 + (8 * cb + q) * 144 + (16 * w + 4 * p) * 2;
            const s16x4 lo = trread(a), hi = trread(a + 4 * 144);
            const u32x2 zl = __builtin_bit_cast(u32x2, lo), zh = __builtin_bit_cast(u32x2, hi);
            const int t = t0 + 16 * w + (lane & 15);
            const u32x4 qq = *(const u32x4*)(bq + (size_t)t * D + c0 + 8 * cb);
            const u32x4 r = {cvtpk(bflo(qq[0]) * bflo(zl[0]), bfhi(qq[0]) * bfhi(zl[0])), cvtpk(bflo(qq[1]) * bflo(zl[1]), bfhi(qq[1]) * bfhi(zl[1])),
                             cvtpk(bflo(qq[2]) * bflo(zh[0]), bfhi(qq[2]) * bfhi(zh[0])), cvtpk(bflo(qq[3]) * bflo(zh[1]), bfhi(qq[3]) * bfhi(zh[1]))};
            *(u32x4*)(out + (size_t)t * OUTW + D + c0 + 8 * cb) = r;
        }
        return;
    }
    const int mblk = b / 9, ng = b - mblk * 9;
#pragma unroll
    for (int i = 0; i < 18; ++i) {
        const int p = w * 18 + i, row = 8 * p + (lane >> 3), ch = (lane & 7) ^ (((row >> 1) & 1) << 2);
        glds16(wo + (size_t)row * D + ng * 64 + ch * 8, lds0 + p * 1024);
    }
    const int tb = mblk * 3 + w;
    const bool active = w < 3 && tb < 64;
    bf16x8 af[36];
    if (active) {
        const char* ap = ws + WS_O + (size_t)tb * 36 * 1024 + lane * 16;
#pragma unroll
        for (int s = 0; s < 36; ++s) af[s] = *(const bf16x8*)(ap + s * 1024);
    }
    asm volatile("s_waitcnt vmcnt(0)\n\ts_barrier" ::: "memory");
    if (!active) return;
    f32x16 acc0 = {}, acc1 = {};
    const int h = lane >> 5, blk = (lane >> 4) & 1, q = (lane & 15) >> 2, p = lane & 3;
#pragma unroll
    for (int s = 0; s < 36; ++s) {
        int r0, r1;
        if (s < 32) { r0 = 18 * s + 4 * h + q; r1 = r0 + 8; }
        else { const int hd = 8 * (s - 32) + 4 * h + (q >> 1); r0 = 18 * hd + 16 + (q & 1); r1 = r0 + 36; }
        const int ch0 = 2 * blk + (p >> 1);
        auto addr = [&](int row, int ch) { return lds0 + row * 128 + ((ch ^ (((row >> 1) & 1) << 2)) << 4) + 8 * (p & 1); };
        const bf16x8 w0 = cat8(trread(addr(r0, ch0)), trread(addr(r1, ch0)));
        const bf16x8 w1 = cat8(trread(addr(r0, ch0 + 4)), trread(addr(r1, ch0 + 4)));
        acc0 = MFMA32(w0, af[s], acc0);
        acc1 = MFMA32(w1, af[s], acc1);
    }
    const int t = tb * 32 + (lane & 31);
    u16* op = out + (size_t)t * OUTW + ng * 64;
#pragma unroll
    for (int g = 0; g < 4; ++g) {
        *(u32x2*)(op + 8 * g + 4 * h) = (u32x2){cvtpk(acc0[4 * g], acc0[4 * g + 1]), cvtpk(acc0[4 * g + 2], acc0[4 * g + 3])};
        *(u32x2*)(op + 32 + 8 * g + 4 * h) = (u32x2){cvtpk(acc1[4 * g], acc1[4 * g + 1]), cvtpk(acc1[4 * g + 2], acc1[4 * g + 3])};
    }
}

extern "C" void kernel_launch(void* const* d_in, const int* in_sizes, int n_in, void* d_out, int out_size, void* d_ws, size_t ws_size,
                              hipStream_t stream) {
    const u16* x = (const u16*)d_in[0];
    const u16* wqkv = (const u16*)d_in[1];
    const u16* wo = (const u16*)d_in[2];
    const u16* bq = (const u16*)d_in[3];
    const u16* bk = (const u16*)d_in[4];
    const u16* bv = (const u16*)d_in[5];
    const u16* shift_k = (const u16*)d_in[6];
    const u16* ssm_k = (const u16*)d_in[7];
    const u16* ssm_D = (const u16*)d_in[8];
    char* ws = (char*)d_ws;
    (void)hipFuncSetAttribute((const void*)k1_qkv, hipFuncAttributeMaxDynamicSharedMemorySize, K1_LDS);
    (void)hipFuncSetAttribute((const void*)k2_attn, hipFuncAttributeMaxDynamicSharedMemorySize, K2_LDS);
    (void)hipFuncSetAttribute((const void*)k_conv, hipFuncAttributeMaxDynamicSharedMemorySize, CV_LDS);
    (void)hipFuncSetAttribute((const void*)k3_out, hipFuncAttributeMaxDynamicSharedMemorySize, K3_LDS);
    k1_qkv<<<256, 512, K1_LDS, stream>>>(x, wqkv, bk, bv, shift_k, ws);
    k2_attn<<<256, 512, K2_LDS, stream>>>(ws);
    k_conv<<<576, 256, CV_LDS, stream>>>(ssm_k, ssm_D, ws);
    k3_out<<<198 + 288, 256, K3_LDS, stream>>>(wo, bq, ws, (u16*)d_out);
}
```

```cpp
#include <hip/hip_runtime.h>
#include <hip/hip_bf16.h>
#include <stdint.h>

#define LAS __attribute__((address_space(3)))
typedef __attribute__((ext_vector_type(8))) __bf16 bf16x8;
typedef __attribute__((ext_vector_type(16))) __bf16 bf16x16;
typedef __attribute__((ext_vector_type(4))) short s16x4;
typedef __attribute__((ext_vector_type(8))) short s16x8;
typedef __attribute__((ext_vector_type(16))) float f32x16;
typedef __attribute__((ext_vector_type(8))) float f32x8;
typedef __attribute__((ext_vector_type(4))) float f32x4;
typedef __attribute__((ext_vector_type(4))) unsigned u32x4;
typedef __attribute__((ext_vector_type(2))) unsigned u32x2;
typedef unsigned short u16;

constexpr int S = 2048, D = 576, NH = 32;
constexpr int OUTW = 2 * D;
constexpr int CH_K2 = 16384, CH_VT = 18432, CH_B = 37888, HEAD_B = 4 * CH_B;
constexpr int QT_B = 1152, QH_B = 64 * QT_B;
constexpr size_t WS_FLAG = 0;
constexpr size_t WS_KV = 8192;
constexpr size_t WS_Q = WS_KV + (size_t)NH * HEAD_B;
constexpr size_t WS_OM = WS_Q + (size_t)NH * QH_B;
constexpr size_t WS_OR = WS_OM + (size_t)128 * 16 * 1024;
constexpr size_t WS_KVT = WS_OR + (size_t)64 * 32 * 128;
constexpr size_t WS_ZT = WS_KVT + (size_t)S * D * 2;
constexpr size_t WS_END = WS_ZT + (size_t)S * D * 2;
constexpr float QSCALE = 0.23570226039551584f * 1.4426950408889634f;
constexpr int LDS_BYTES = 158720;

#define STAMP(kid, pt)
#define SBAR() __builtin_amdgcn_sched_barrier(0)
#define MFMA32(a, b, c) __builtin_amdgcn_mfma_f32_32x32x16_bf16(a, b, c, 0, 0, 0)
#define MFMA16(a, b, c) __builtin_amdgcn_mfma_f32_16x16x32_bf16(a, b, c, 0, 0, 0)
#define WAITBAR(V) asm volatile("s_waitcnt vmcnt(" #V ") lgkmcnt(0)\n\ts_barrier" ::: "memory")
#define LBAR() asm volatile("s_waitcnt lgkmcnt(0)\n\ts_barrier" ::: "memory")

__device__ __forceinline__ void glds16(const void* g, unsigned lds_base) {
    unsigned sv;
    asm volatile("s_mov_b32 %0, m0\n\ts_mov_b32 m0, %2\n\ts_nop 0\n\tglobal_load_lds_dwordx4 %1, off\n\ts_mov_b32 m0, %0"
                 : "=&s"(sv) : "v"(g), "s"(lds_base) : "memory");
}
__device__ __forceinline__ unsigned cvtpk(float lo, float hi) {
    typedef __attribute__((ext_vector_type(2))) float f2; typedef __attribute__((ext_vector_type(2))) __bf16 b2;
    f2 v = {lo, hi};
    return __builtin_bit_cast(unsigned, __builtin_convertvector(v, b2));
}
__device__ __forceinline__ float bflo(unsigned u) { return __uint_as_float(u << 16); }
__device__ __forceinline__ float bfhi(unsigned u) { return __uint_as_float(u & 0xffff0000u); }
__device__ __forceinline__ float rbf(float x) { return bflo(cvtpk(x, 0.f)); }
__device__ __forceinline__ s16x4 trread(unsigned a) {
    return __builtin_bit_cast(s16x4, __builtin_amdgcn_ds_read_tr16_b64_v4i16((LAS s16x4*)(uintptr_t)a));
}
__device__ __forceinline__ bf16x8 cat8(s16x4 a, s16x4 b) { return __builtin_bit_cast(bf16x8, (s16x8)__builtin_shufflevector(a, b, 0, 1, 2, 3, 4, 5, 6, 7)); }
__device__ __forceinline__ bf16x8 ldsrd128(unsigned a) { return *(LAS const bf16x8*)(uintptr_t)a; }
__device__ __forceinline__ u32x4 ldsrdu4(unsigned a) { return *(LAS const u32x4*)(uintptr_t)a; }
__device__ __forceinline__ u32x2 ldsrdu2(unsigned a) { return *(LAS const u32x2*)(uintptr_t)a; }
__device__ __forceinline__ void ldswr4(unsigned a, u32x4 v) { *(LAS u32x4*)(uintptr_t)a = v; }
__device__ __forceinline__ void ldswr2(unsigned a, u32x2 v) { *(LAS u32x2*)(uintptr_t)a = v; }
__device__ __forceinline__ void ldswr1(unsigned a, unsigned v) { *(LAS unsigned*)(uintptr_t)a = v; }

typedef __amdgpu_buffer_rsrc_t rsrc_t;
__device__ __forceinline__ rsrc_t ws_rsrc(char* ws) { return __builtin_amdgcn_make_buffer_rsrc(ws, 0, (int)WS_END, 0x00020000); }
__device__ __forceinline__ void st16(rsrc_t r, unsigned off, u32x4 v) { __builtin_amdgcn_raw_buffer_store_b128(v, r, off, 0, 16); }
__device__ __forceinline__ void st8(rsrc_t r, unsigned off, u32x2 v) { __builtin_amdgcn_raw_buffer_store_b64(v, r, off, 0, 16); }
__device__ __forceinline__ void st4(rsrc_t r, unsigned off, unsigned v) { __builtin_amdgcn_raw_buffer_store_b32(v, r, off, 0, 16); }
__device__ __forceinline__ u32x4 ld16(rsrc_t r, unsigned off) { return __builtin_amdgcn_raw_buffer_load_b128(r, off, 0, 16); }
__device__ __forceinline__ unsigned ld4(rsrc_t r, unsigned off) { return __builtin_amdgcn_raw_buffer_load_b32(r, off, 0, 16); }

constexpr int KVS_K = 0, KVS_W = 67 * 144, KVS_T = KVS_W + 512, KVS_B = KVS_T + 64 * 144;
struct KvRegs { u32x4 k, v, x; };
__device__ __forceinline__ u32x4 gload16(const void* p) {
    u32x4 r; asm volatile("global_load_dwordx4 %0, %1, off" : "=v"(r) : "v"(p) : "memory"); return r;
}
__device__ __forceinline__ void kv_issue(KvRegs& R, const u16* __restrict__ bk, const u16* __restrict__ bv, const u16* __restrict__ shift_k, int tb, int cg) {
    const int tid = threadIdx.x, t = tb * 64 + (tid >> 3), c = cg * 64 + (tid & 7) * 8;
    R.k = gload16(bk + (size_t)t * D + c);
    R.v = gload16(bv + (size_t)t * D + c);
    const u16* xp = bk + c;
    if (tid < 24) { const int th = tb * 64 - 3 + (tid >> 3); xp = bk + (size_t)(th < 0 ? 0 : th) * D + c; }
    else if (tid >= 64 && tid < 96) xp = shift_k + cg * 256 + (tid - 64) * 8;
    R.x = gload16(xp);
}
#define KV_WAIT(R, N) asm volatile("s_waitcnt vmcnt(" #N ")" : "+v"(R.k), "+v"(R.v), "+v"(R.x) :: "memory")
__device__ __forceinline__ void kv_stage(const KvRegs& R, int tb, unsigned sc) {
    const int tid = threadIdx.x;
    ldswr4(sc + KVS_K + ((tid >> 3) + 3) * 144 + (tid & 7) * 16, R.k);
    if (tid < 24) ldswr4(sc + KVS_K + (tid >> 3) * 144 + (tid & 7) * 16, (tb * 64 - 3 + (tid >> 3) < 0) ? (u32x4){0u, 0u, 0u, 0u} : R.x);
    else if (tid >= 64 && tid < 96) ldswr4(sc + KVS_W + (tid - 64) * 16, R.x);
}
__device__ __forceinline__ unsigned kv_pair(unsigned k0, unsigned k1, unsigned k2, unsigned k3, unsigned vv, unsigned wa0, unsigned wa1, unsigned wb0, unsigned wb1) {
    const unsigned p0 = cvtpk(bflo(k0) * bfhi(wa1), bfhi(k0) * bfhi(wb1));
    const unsigned p1 = cvtpk(bflo(k1) * bflo(wa1), bfhi(k1) * bflo(wb1));
    unsigned s = cvtpk(bflo(p0) + bflo(p1), bfhi(p0) + bfhi(p1));
    const unsigned p2 = cvtpk(bflo(k2) * bfhi(wa0), bfhi(k2) * bfhi(wb0));
    s = cvtpk(bflo(s) + bflo(p2), bfhi(s) + bfhi(p2));
    const unsigned p3 = cvtpk(bflo(k3) * bflo(wa0), bfhi(k3) * bflo(wb0));
    s = cvtpk(bflo(s) + bflo(p3), bfhi(s) + bfhi(p3));
    return cvtpk(bflo(s) * bflo(vv), bfhi(s) * bfhi(vv));
}
__device__ __forceinline__ void kv_compute(const KvRegs& R, unsigned sc) {
    const int tid = threadIdx.x, trow = tid >> 3, ch = tid & 7;
    const unsigned ka = sc + KVS_K + trow * 144 + ch * 16;
    const u32x4 k0 = ldsrdu4(ka), k1 = ldsrdu4(ka + 144), k2 = ldsrdu4(ka + 288);
    const unsigned wa = sc + KVS_W + ch * 64;
    const u32x4 w0 = ldsrdu4(wa), w1 = ldsrdu4(wa + 16), w2 = ldsrdu4(wa + 32), w3 = ldsrdu4(wa + 48);
    const u32x4 r = {kv_pair(k0[0], k1[0], k2[0], R.k[0], R.v[0], w0[0], w0[1], w0[2], w0[3]),
                     kv_pair(k0[1], k1[1], k2[1], R.k[1], R.v[1], w1[0], w1[1], w1[2], w1[3]),
                     kv_pair(k0[2], k1[2], k2[2], R.k[2], R.v[2], w2[0], w2[1], w2[2], w2[3]),
                     kv_pair(k0[3], k1[3], k2[3], R.k[3], R.v[3], w3[0], w3[1], w3[2], w3[3])};
    const int row = (trow & ~7) | (7 - (trow & 7));
    ldswr4(sc + KVS_T + row * 144 + ch * 16, r);
}
__device__ __forceinline__ void kv_store(rsrc_t rs, int tb, int cg, unsigned sc, int w, int lane) {
    const int G = lane >> 4, q = (lane & 15) >> 2, p = lane & 3;
    const int cw = (w >> 1) * 16;
    s16x4 a[4];
#pragma unroll
    for (int u = 0; u < 4; ++u) a[u] = trread(sc + KVS_T + (16 * G + 4 * u + q) * 144 + (cw + 4 * p) * 2);
    const int i = lane & 15;
    if ((i >> 3) == (w & 1)) {
        const unsigned off = (unsigned)WS_KVT + (unsigned)(cg * 64 + cw + i) * (S * 2) + (tb * 64 + 16 * G) * 2;
        st16(rs, off, __builtin_bit_cast(u32x4, (s16x8)__builtin_shufflevector(a[0], a[1], 0, 1, 2, 3, 4, 5, 6, 7)));
        st16(rs, off + 16, __builtin_bit_cast(u32x4, (s16x8)__builtin_shufflevector(a[2], a[3], 0, 1, 2, 3, 4, 5, 6, 7)));
    }
}

constexpr int G1_STAGE = 32768, G1_SC = 4 * G1_STAGE;

template <bool ISV>
__device__ __forceinline__ void qkv_tile(const u16* __restrict__ x, const u16* __restrict__ wqkv, char* __restrict__ ws, rsrc_t rs,
                                         int mb, int ng, unsigned lds0, int w, int lane,
                                         const u16* __restrict__ bk, const u16* __restrict__ bv, const u16* __restrict__ shift_k, int kvtb, int kvcg) {
    const int ctl = w & 3, tb0 = (w >> 2) * 2;
    const bool active = (ng * 4 + ctl) < 54;
    const u16* xsrc[2]; const u16* wsrc[2];
#pragma unroll
    for (int i = 0; i < 2; ++i) {
        const int p = w * 2 + i;
        const int xr = 8 * p + (lane >> 3), xc = (lane & 7) ^ ((xr >> 1) & 7);
        xsrc[i] = x + (size_t)(mb * 128 + xr) * D + xc * 8;
        const int wr = 4 * p + (lane >> 4), wc = (lane & 15) ^ (((wr & 3) << 2) | ((wr >> 2) & 3));
        int col = ng * 128 + wc * 8; col = col > 1720 ? 1720 : col;
        wsrc[i] = wqkv + (size_t)wr * (3 * D) + col;
    }
    auto issue1 = [&](int c, int pc) {
        const unsigned st = lds0 + (c & 3) * G1_STAGE;
        const int i = pc >> 1;
        if (pc & 1) glds16(wsrc[i] + (size_t)c * 64 * (3 * D), st + 16384 + (w * 2 + i) * 1024);
        else glds16(xsrc[i] + c * 64, st + (w * 2 + i) * 1024);
    };
    auto issue = [&](int c) { issue1(c, 0); issue1(c, 1); issue1(c, 2); issue1(c, 3); };
    const int r = lane & 31, h = lane >> 5;
    unsigned xa[4];
#pragma unroll
    for (int s = 0; s < 4; ++s) xa[s] = lds0 + (tb0 * 32 + r) * 128 + (((2 * s + h) ^ ((r >> 1) & 7)) << 4);
    unsigned wa[2];
    {   const int blk = (lane >> 4) & 1, q = (lane & 15) >> 2, p = lane & 3;
#pragma unroll
        for (int u = 0; u < 2; ++u) {
            const int row = 8 * h + 4 * u + q, ch = 4 * ctl + 2 * blk + (p >> 1);
            wa[u] = lds0 + 16384 + row * 256 + ((ch ^ (((row & 3) << 2) | ((row >> 2) & 3))) << 4) + 8 * (p & 1);
        } }
    f32x16 acc0 = {}, acc1 = {};
    KvRegs R;
    issue(0);
    kv_issue(R, bk, bv, shift_k, kvtb, kvcg);
    issue(1); issue(2); issue(3);
    STAMP(0, 1);
    struct Frags { bf16x8 w[4], x0[4], x1[4]; };
    Frags fa, fb;
    auto rd = [&](Frags& f, int c, int s) {
        const unsigned st = (c & 3) * G1_STAGE;
        f.w[s] = cat8(trread(wa[0] + st + s * 4096), trread(wa[1] + st + s * 4096));
        f.x0[s] = ldsrd128(xa[s] + st); f.x1[s] = ldsrd128(xa[s] + st + 4096);
    };
    auto mm = [&](const Frags& f, int s) {
        if (ISV) { acc0 = MFMA32(f.x0[s], f.w[s], acc0); acc1 = MFMA32(f.x1[s], f.w[s], acc1); }
        else     { acc0 = MFMA32(f.w[s], f.x0[s], acc0); acc1 = MFMA32(f.w[s], f.x1[s], acc1); }
    };
#define G1_STEP(CUR, NXT, c, N) do { WAITBAR(N); SBAR(); \
        if (active) { _Pragma("unroll") for (int s = 0; s < 4; ++s) { \
              if ((c) + 4 < 9 && s < 2) { issue1((c) + 4, 2 * s); issue1((c) + 4, 2 * s + 1); } SBAR(); rd(NXT, (c) + 1, s); SBAR(); mm(CUR, s); SBAR(); } } \
        else if ((c) + 4 < 9) issue((c) + 4); } while (0)
    const unsigned sc = lds0 + G1_SC;
    KV_WAIT(R, 12);
    kv_stage(R, kvtb, sc);
    WAITBAR(12);
    if (active) { _Pragma("unroll") for (int s = 0; s < 4; ++s) rd(fa, 0, s); }
    kv_compute(R, sc);
    G1_STEP(fa, fb, 0, 8);
    kv_store(rs, kvtb, kvcg, sc, w, lane);
    STAMP(0, 2);
    G1_STEP(fb, fa, 1, 10);
    G1_STEP(fa, fb, 2, 10);
    G1_STEP(fb, fa, 3, 8);
    G1_STEP(fa, fb, 4, 8);
    G1_STEP(fb, fa, 5, 8);
    G1_STEP(fa, fb, 6, 4);
    G1_STEP(fb, fa, 7, 0);
    if (active) { _Pragma("unroll") for (int s = 0; s < 4; ++s) mm(fa, s); }
    STAMP(0, 3);
    const int ct = ng * 4 + ctl;
    if (ISV) {
        if (active) {
#pragma unroll
            for (int i = 0; i < 2; ++i) {
                const f32x16& acc = i ? acc1 : acc0;
                const int T = mb * 4 + tb0 + i;
                const int col = (ct - 36) * 32 + r, head = col / 18, d = col - head * 18;
                const unsigned vp = (unsigned)WS_KV + head * HEAD_B + (((T >> 4) & 1) * 2 + (T >> 5)) * CH_B + CH_VT + (2 * (T & 15)) * 608 + h * 304 + d * 16;
#pragma unroll
                for (int g = 0; g < 2; ++g) {
                    const u32x4 o = {cvtpk(acc[8 * g], acc[8 * g + 1]), cvtpk(acc[8 * g + 2], acc[8 * g + 3]),
                                     cvtpk(acc[8 * g + 4], acc[8 * g + 5]), cvtpk(acc[8 * g + 6], acc[8 * g + 7])};
                    st16(rs, vp + g * 608, o);
                    if (d == 17) st16(rs, vp + g * 608 + 16, (u32x4){0x3f803f80u, 0x3f803f80u, 0x3f803f80u, 0x3f803f80u});
                }
            }
        }
    } else {
        LBAR();
        if (active) {
            const bool isq = ct < 18;
            const float scl = isq ? QSCALE : 1.0f;
            const int row = isq ? r : ((r & 16) | ((r & 4) << 1) | ((r & 8) >> 1) | (r & 3));
#pragma unroll
            for (int i = 0; i < 2; ++i) {
                const f32x16& acc = i ? acc1 : acc0;
#pragma unroll
                for (int g = 0; g < 4; ++g)
                    ldswr2(lds0 + (tb0 + i) * 8192 + (8 * ctl + 2 * g + h) * 256 + row * 8,
                           (u32x2){cvtpk(acc[4 * g] * scl, acc[4 * g + 1] * scl), cvtpk(acc[4 * g + 2] * scl, acc[4 * g + 3] * scl)});
            }
        }
        LBAR();
        const int g0 = ng * 32;
        for (int it = w; it < 4 * 10; it += 8) {
            const int tt = it / 10, cand = (it % 10) * 2 + h;
            const int blk0 = g0 / 9, blk = blk0 + (cand >> 2), cpos = cand & 3;
            const int pos = (cpos & 1) * 2 + (cpos >> 1) * 5;
            const int gq = blk * 9 + pos, lq = gq - g0;
            const bool in0 = lq >= 0 && lq < 32 && gq < 288, in1 = lq + 1 >= 0 && lq + 1 < 32 && gq + 1 < 288;
            if (in0 || in1) {
                const bool isq = gq < 144;
                const int pb = isq ? blk : blk - 16, head = 2 * pb + (cpos >> 1), hp = cpos & 1;
                const int T = mb * 4 + tt;
                const unsigned dst = isq ? (unsigned)WS_Q + head * QH_B + T * QT_B + hp * 512 + r * 16
                                         : (unsigned)WS_KV + head * HEAD_B + (((T >> 4) & 1) * 2 + (T >> 5)) * CH_B + (T & 15) * 1024 + hp * 512 + r * 16;
                const unsigned sa = lds0 + tt * 8192 + lq * 256 + r * 8;
                if (in0 && in1) { const u32x2 a = ldsrdu2(sa), b = ldsrdu2(sa + 256); st16(rs, dst, (u32x4){a[0], a[1], b[0], b[1]}); }
                else if (in0) st8(rs, dst, ldsrdu2(sa));
                else st8(rs, dst + 8, ldsrdu2(sa + 256));
            }
        }
        {
            const int blk = g0 / 9 + w, gq = blk * 9 + 4, lq = gq - g0;
            if (w < 5 && lq >= 0 && lq < 32 && gq < 288) {
                const int tt = lane >> 4, par = (lane >> 3) & 1, rq = lane & 7;
                const bool isq = gq < 144;
                const int head = 2 * (isq ? blk : blk - 16) + par, T = mb * 4 + tt;
                const unsigned sa = lds0 + tt * 8192 + lq * 256 + rq * 32 + par * 4;
                const u32x4 v = {*(LAS const unsigned*)(uintptr_t)sa, *(LAS const unsigned*)(uintptr_t)(sa + 8),
                                 *(LAS const unsigned*)(uintptr_t)(sa + 16), *(LAS const unsigned*)(uintptr_t)(sa + 24)};
                const unsigned dst = isq ? (unsigned)WS_Q + head * QH_B + T * QT_B + 1024 + rq * 16
                                         : (unsigned)WS_KV + head * HEAD_B + (((T >> 4) & 1) * 2 + (T >> 5)) * CH_B + CH_K2 + (T & 15) * 128 + rq * 16;
                st16(rs, dst, v);
            }
        }
    }
}

__device__ __forceinline__ void phase1(const u16* x, const u16* wqkv, const u16* bk, const u16* bv, const u16* shift_k, char* ws, rsrc_t rs,
                                       unsigned lds0, int w, int lane) {
    const int b = blockIdx.x;
    if (b >= 224) {
        const int t = 224 + (b - 224) * 2;
        KvRegs R0, R1;
        kv_issue(R0, bk, bv, shift_k, t & 31, t >> 5); kv_issue(R1, bk, bv, shift_k, (t + 1) & 31, (t + 1) >> 5);
        KV_WAIT(R0, 3); kv_stage(R0, t & 31, lds0);
        KV_WAIT(R1, 0); kv_stage(R1, (t + 1) & 31, lds0 + KVS_B);
        LBAR();
        kv_compute(R0, lds0); kv_compute(R1, lds0 + KVS_B);
        LBAR();
        kv_store(rs, t & 31, t >> 5, lds0, w, lane); kv_store(rs, (t + 1) & 31, (t + 1) >> 5, lds0 + KVS_B, w, lane);
        return;
    }
    const int xcd = b & 7, i = b >> 3, mb = (xcd >> 1) * 4 + (i & 3), ng = (xcd & 1) * 7 + (i >> 2);
    if (ng >= 9) qkv_tile<true>(x, wqkv, ws, rs, mb, ng, lds0, w, lane, bk, bv, shift_k, b & 31, b >> 5);
    else qkv_tile<false>(x, wqkv, ws, rs, mb, ng, lds0, w, lane, bk, bv, shift_k, b & 31, b >> 5);
}

constexpr int CV_U = 33280, CV_B = CV_U + 7600;
constexpr int CV_BASE = LDS_BYTES - 2 * CV_B;

struct ConvRegs { u32x4 h0, h1; unsigned h2; u32x4 u; };
__device__ __forceinline__ void conv_issue(ConvRegs& R, const u16* __restrict__ ssm_k, rsrc_t rs, int c) {
    const int i = threadIdx.x & 255;
    const rsrc_t hr = __builtin_amdgcn_make_buffer_rsrc((void*)(ssm_k + (size_t)c * S), 0, S * 2, 0x00020000);
    const unsigned off = i < 4 ? 0u : (unsigned)(((9 * i - 32) >> 1) * 4);
    R.h0 = __builtin_amdgcn_raw_buffer_load_b128(hr, off, 0, 0);
    R.h1 = __builtin_amdgcn_raw_buffer_load_b128(hr, off + 16, 0, 0);
    R.h2 = __builtin_amdgcn_raw_buffer_load_b32(hr, off + 32, 0, 0);
    R.u = ld16(rs, (unsigned)WS_KVT + (unsigned)c * (S * 2) + i * 16);
}
__device__ __forceinline__ void conv_build(const ConvRegs& R, unsigned base) {
    const int i = threadIdx.x & 255;
    if (i < 4) {
        const unsigned dw[8] = {R.h0[0], R.h0[1], R.h0[2], R.h0[3], R.h1[0], R.h1[1], R.h1[2], R.h1[3]};
#pragma unroll
        for (int u = 0; u < 9; ++u) {
            const int x = 9 * i - 32 + u;
            unsigned e[4];
#pragma unroll
            for (int j = 0; j < 4; ++j) {
                unsigned lo = 0, hi = 0;
#pragma unroll
                for (int n = 0; n < 16; ++n) { const unsigned v = (n & 1) ? dw[n >> 1] >> 16 : dw[n >> 1] & 0xffffu; if (x + 2 * j == n) lo = v; if (x + 2 * j + 1 == n) hi = v; }
                e[j] = lo | (hi << 16);
            }
            ldswr4(base + (9 * i + u) * 16, (u32x4){e[0], e[1], e[2], e[3]});
        }
    } else if (i < 232) {
        const unsigned sh = (i & 1) * 16;
        const unsigned dw[9] = {R.h0[0], R.h0[1], R.h0[2], R.h0[3], R.h1[0], R.h1[1], R.h1[2], R.h1[3], R.h2};
        unsigned A[8], Bq[7];
#pragma unroll
        for (int n = 0; n < 8; ++n) A[n] = __builtin_amdgcn_alignbit(dw[n + 1], dw[n], sh);
#pragma unroll
        for (int n = 0; n < 7; ++n) Bq[n] = __builtin_amdgcn_alignbit(A[n + 1], A[n], 16);
        const unsigned a = base + 9 * i * 16;
#pragma unroll
        for (int u = 0; u < 9; ++u) {
            if (i < 231 || u < 1) {
                if (u & 1) ldswr4(a + u * 16, (u32x4){Bq[u >> 1], Bq[(u >> 1) + 1], Bq[(u >> 1) + 2], Bq[(u >> 1) + 3]});
                else ldswr4(a + u * 16, (u32x4){A[u >> 1], A[(u >> 1) + 1], A[(u >> 1) + 2], A[(u >> 1) + 3]});
            }
        }
    }
    ldswr4(base + CV_U + (31 + (i >> 2)) * 80 + (i & 3) * 16, R.u);
    if (i < 155) ldswr4(base + CV_U + i * 16, (u32x4){0u, 0u, 0u, 0u});
}
template <int JB, int JE>
__device__ __forceinline__ void conv_range(unsigned aaddr, unsigned baddr, f32x16& d0, f32x16& d1) {
    auto boff = [](int j) { const int c2 = 1040 - 16 * j; return ((c2 >> 5) + 31) * 80 + (c2 & 16) * 2; };
    bf16x8 a = ldsrd128(aaddr + JB * 256), b1 = ldsrd128(baddr + boff(JB)), b0 = ldsrd128(baddr + boff(JB) - (JB < 64 ? 2560 : 0));
#pragma unroll
    for (int j = JB; j < JE; ++j) {
        bf16x8 an = a, b1n = b1, b0n = b0;
        if (j + 1 < JE) { an = ldsrd128(aaddr + (j + 1) * 256); b1n = ldsrd128(baddr + boff(j + 1)); if (j + 1 < 64) b0n = ldsrd128(baddr + boff(j + 1) - 2560); }
        SBAR();
        d1 = MFMA32(a, b1, d1);
        if (j < 64) d0 = MFMA32(a, b0, d0);
        SBAR();
        a = an; b1 = b1n; b0 = b0n;
    }
}
__device__ __forceinline__ void conv_main(unsigned base, int wg, int lane, f32x16& d0, f32x16& d1) {
    const int n = lane & 31, hh = lane >> 5;
    const unsigned aaddr = base + (n - 8 * hh + 9) * 16, baddr = base + CV_U + n * 80 + hh * 16;
    d0 = f32x16{}; d1 = f32x16{};
    if (wg == 0) conv_range<0, 24>(aaddr, baddr, d0, d1);
    else if (wg == 1) conv_range<24, 48>(aaddr, baddr, d0, d1);
    else if (wg == 2) conv_range<48, 80>(aaddr, baddr, d0, d1);
    else conv_range<80, 128>(aaddr, baddr, d0, d1);
}
__device__ __forceinline__ void conv_partials(unsigned base, int wg, int lane, const f32x16& d0, const f32x16& d1) {
    const int n = lane & 31, hh = lane >> 5;
    const unsigned p = base + wg * 9216 + (36 * n + 4 * hh) * 4;
#pragma unroll
    for (int g = 0; g < 4; ++g) {
        if (wg < 3) *(LAS f32x4*)(uintptr_t)(p + g * 32) = (f32x4){d0[4 * g], d0[4 * g + 1], d0[4 * g + 2], d0[4 * g + 3]};
        *(LAS f32x4*)(uintptr_t)(p + 4608 + g * 32) = (f32x4){d1[4 * g], d1[4 * g + 1], d1[4 * g + 2], d1[4 * g + 3]};
    }
}
__device__ __forceinline__ void conv_finish(unsigned base, rsrc_t rs, const u16* __restrict__ ssm_D, int c, const u32x4& uu) {
    const int i = threadIdx.x & 255;
    const unsigned p = base + (8 * i + 4 * (i >> 2)) * 4;
    f32x4 lo = *(LAS const f32x4*)(uintptr_t)p, hi = *(LAS const f32x4*)(uintptr_t)(p + 16);
#pragma unroll
    for (int k = 1; k < 4; ++k)
        if (k < 3 || i >= 128) { lo += *(LAS const f32x4*)(uintptr_t)(p + k * 9216); hi += *(LAS const f32x4*)(uintptr_t)(p + k * 9216 + 16); }
    const float dd = bflo(ssm_D[c]);
    const float y[8] = {lo[0], lo[1], lo[2], lo[3], hi[0], hi[1], hi[2], hi[3]};
    unsigned o[4];
#pragma unroll
    for (int j = 0; j < 4; ++j) {
        const float u0 = bfhi(uu[3 - j]), u1 = bflo(uu[3 - j]);
        o[j] = cvtpk(rbf(y[2 * j]) + rbf(u0 * dd), rbf(y[2 * j + 1]) + rbf(u1 * dd));
    }
    st16(rs, (unsigned)WS_ZT + (unsigned)c * (S * 2) + i * 16, (u32x4){o[0], o[1], o[2], o[3]});
}
__device__ __forceinline__ void conv_pair(const u16* ssm_k, const u16* ssm_D, rsrc_t rs, int pair, unsigned lds0, int w, int lane) {
    const int grp = w >> 2, c = 2 * pair + grp;
    const unsigned base = lds0 + CV_BASE + grp * CV_B;
    ConvRegs R;
    conv_issue(R, ssm_k, rs, c);
    conv_build(R, base);
    LBAR();
    f32x16 d0, d1;
    conv_main(base, w & 3, lane, d0, d1);
    LBAR();
    conv_partials(base, w & 3, lane, d0, d1);
    LBAR();
    conv_finish(base, rs, ssm_D, c, R.u);
}

__device__ __forceinline__ unsigned attn_tile_off(int t) { return t < 16 ? 0u : 2u * CH_B; }
__device__ __forceinline__ void attn_slow(unsigned base, int lane, bf16x8 qf, s16x4 q2, f32x16& o, float& mrow) {
    const int m_ = lane & 31, h = lane >> 5, vrow = (m_ < 19) ? m_ : ((m_ == 22) ? 18 : 0);
    float m = -3.0e38f;
    o = f32x16{};
    for (int t = 0; t < 32; ++t) {
        const unsigned cb = base + attn_tile_off(t), tl = t & 15;
        const bf16x8 kf = ldsrd128(cb + tl * 1024 + lane * 16);
        const unsigned k2 = *(LAS const unsigned*)(uintptr_t)(cb + CH_K2 + tl * 128 + m_ * 4);
        f32x16 s = {};
        s = MFMA32(kf, qf, s);
        s = __builtin_amdgcn_mfma_f32_32x32x8bf16_1k(__builtin_bit_cast(s16x4, (u32x2){k2, 0u}), q2, s, 0, 0, 0);
        float tm = s[0];
#pragma unroll
        for (int r = 1; r < 16; ++r) tm = fmaxf(tm, s[r]);
        tm = fmaxf(tm, __shfl_xor(tm, 32));
        const float mn = fmaxf(m, tm);
        const float f = __builtin_amdgcn_exp2f(m - mn);
        m = mn;
#pragma unroll
        for (int r = 0; r < 16; ++r) { o[r] *= f; s[r] = __builtin_amdgcn_exp2f(s[r] - mn); }
        const f32x8 lo = __builtin_shufflevector(s, s, 0, 1, 2, 3, 4, 5, 6, 7), hi = __builtin_shufflevector(s, s, 8, 9, 10, 11, 12, 13, 14, 15);
        const unsigned va = cb + CH_VT + tl * 1216 + h * 304 + vrow * 16;
        o = __builtin_amdgcn_smfmac_f32_32x32x32_bf16(ldsrd128(va), __builtin_bit_cast(bf16x16, lo), o, (int)0xDDDDDDDD, 0, 0);
        o = __builtin_amdgcn_smfmac_f32_32x32x32_bf16(ldsrd128(va + 608), __builtin_bit_cast(bf16x16, hi), o, (int)0xDDDDDDDD, 0, 0);
    }
    mrow = m;
}
__device__ __forceinline__ bool attn_range_ok(const f32x16& o) {
    const float l = o[10];
    bool ok = (l >= 5.9604645e-8f) && (l <= 1.8446744e19f);
#pragma unroll
    for (int r = 0; r < 10; ++r) ok = ok && (fabsf(o[r]) <= 3.0e38f);
    return __all(ok);
}
__device__ __forceinline__ void attn_dma(const char* ws, int head, int first, unsigned lds0, int w, int lane) {
    const char* src = ws + WS_KV + (size_t)head * HEAD_B + first * CH_B + lane * 16;
#pragma unroll
    for (int i = 0; i < 10; ++i) { int p = w + 8 * i; p = p > 73 ? 73 : p; glds16(src + p * 1024, lds0 + first * CH_B + p * 1024); }
}
struct AttnQ { bf16x8 fA, fB; s16x4 rA, rB; unsigned a, b; };
__device__ __forceinline__ void attn_loadq(AttnQ& Q, const char* ws, int head, int qtA, int lane) {
    const char* qp = ws + WS_Q + (size_t)head * QH_B + (size_t)qtA * QT_B;
    asm volatile("global_load_dwordx4 %0, %1, off sc1" : "=v"(Q.fA) : "v"(qp + lane * 16) : "memory");
    asm volatile("global_load_dwordx4 %0, %1, off sc1" : "=v"(Q.fB) : "v"(qp + QT_B + lane * 16) : "memory");
    asm volatile("global_load_dword %0, %1, off sc1" : "=v"(Q.a) : "v"(qp + 1024 + (lane & 31) * 4) : "memory");
    asm volatile("global_load_dword %0, %1, off sc1" : "=v"(Q.b) : "v"(qp + QT_B + 1024 + (lane & 31) * 4) : "memory");
}
#define ATTN_QWAITBAR(Q, N) asm volatile("s_waitcnt vmcnt(" #N ") lgkmcnt(0)\n\ts_barrier" : "+v"(Q.fA), "+v"(Q.fB), "+v"(Q.a), "+v"(Q.b) :: "memory")
__device__ __forceinline__ void attn_fixq(AttnQ& Q, int lane) {
    Q.rA = __builtin_bit_cast(s16x4, (u32x2){lane < 32 ? Q.a : 0u, 0u}); Q.rB = __builtin_bit_cast(s16x4, (u32x2){lane < 32 ? Q.b : 0u, 0u});
}

__device__ __forceinline__ void attn_loop(AttnQ& Q, unsigned lds0, int w, int lane, f32x16& oA, f32x16& oB) {
    const int kpart = w >> 2, m = lane & 31, h = lane >> 5;
    const int vrow = (m < 19) ? m : ((m == 22) ? 18 : 0);
    unsigned ka = lds0 + kpart * CH_B + lane * 16, k2a = lds0 + kpart * CH_B + CH_K2 + m * 4, va = lds0 + kpart * CH_B + CH_VT + h * 304 + vrow * 16;
    unsigned idx = 0xDDDDDDDDu;
    int cnt = 16;
    const unsigned long long vmask = (((1ull << 19) - 1) | (1ull << 22)) * 0x100000001ull;
    oA = f32x16{}; oB = f32x16{};
#define EXP4(d0, d1, d2, d3, s0, s1, s2, s3) \
        "v_exp_f32 v" #d0 ", v" #s0 "\n v_exp_f32 v" #d1 ", v" #s1 "\n v_exp_f32 v" #d2 ", v" #s2 "\n v_exp_f32 v" #d3 ", v" #s3 "\n"
#define VRD(x) "s_mov_b64 exec, %11\n" x "s_mov_b64 exec, -1\n"
#define KEYTILE(PK, OK, PK2, OK2, OVH, PV, OVL, PL, PH) \
        "v_mfma_f32_32x32x16_bf16 v[80:95], v[112:115], %3, 0\n" \
        PK "ds_read_b128 v[112:115], %7 offset:" #OK "\n" \
        PL EXP4(96, 97, 98, 99, 64, 65, 66, 67) PH \
        "v_mfma_f32_32x32x8_bf16 v[80:95], v[116:117], %5, v[80:95]\n" \
        PK2 "ds_read_b32 v116, %8 offset:" #OK2 "\n" \
        EXP4(100, 101, 102, 103, 68, 69, 70, 71) \
        "v_smfmac_f32_32x32x32_bf16 %1, v[122:125], v[104:111], %6\n" \
        VRD("ds_read_b128 v[122:125], %9 offset:" #OVH "\n") \
        EXP4(104, 105, 106, 107, 72, 73, 74, 75) \
        "s_waitcnt lgkmcnt(3)\n" \
        "v_smfmac_f32_32x32x32_bf16 %0, v[118:121], v[96:103], %6\n" \
        EXP4(108, 109, 110, 111, 76, 77, 78, 79) \
        "s_waitcnt lgkmcnt(2)\n" \
        "v_mfma_f32_32x32x16_bf16 v[64:79], v[112:115], %2, 0\n" \
        PL EXP4(96, 97, 98, 99, 80, 81, 82, 83) PH \
        "s_waitcnt lgkmcnt(1)\n" \
        "v_mfma_f32_32x32x8_bf16 v[64:79], v[116:117], %4, v[64:79]\n" \
        EXP4(100, 101, 102, 103, 84, 85, 86, 87) \
        "s_waitcnt lgkmcnt(0)\n" \
        "v_smfmac_f32_32x32x32_bf16 %0, v[122:125], v[104:111], %6\n" \
        EXP4(104, 105, 106, 107, 88, 89, 90, 91) \
        "v_smfmac_f32_32x32x32_bf16 %1, v[118:121], v[96:103], %6\n" \
        PV VRD("ds_read_b128 v[118:121], %9 offset:" #OVL "\n") \
        EXP4(108, 109, 110, 111, 92, 93, 94, 95)
#define LOOPASM(PL, PH, PRE) \
    asm volatile( \
        PRE \
        "ds_read_b128 v[112:115], %7\n" \
        "ds_read_b32 v116, %8\n" \
        "v_mov_b32 v117, 0\n" \
        "v_mov_b32 v104, 0\n v_mov_b32 v105, 0\n v_mov_b32 v106, 0\n v_mov_b32 v107, 0\n" \
        "v_mov_b32 v108, 0\n v_mov_b32 v109, 0\n v_mov_b32 v110, 0\n v_mov_b32 v111, 0\n" \
        "v_mov_b32 v122, 0\n v_mov_b32 v123, 0\n v_mov_b32 v124, 0\n v_mov_b32 v125, 0\n" \
        "v_mov_b32 v118, 0\n v_mov_b32 v119, 0\n v_mov_b32 v120, 0\n v_mov_b32 v121, 0\n" \
        "s_waitcnt lgkmcnt(0)\n" \
        "v_mfma_f32_32x32x16_bf16 v[64:79], v[112:115], %2, 0\n" \
        "v_mfma_f32_32x32x8_bf16 v[64:79], v[116:117], %4, v[64:79]\n" \
        VRD("ds_read_b128 v[118:121], %9\n") \
        "s_nop 7\n s_nop 7\n" \
        "1:\n" \
        "s_movk_i32 s40, 2048\n s_movk_i32 s41, 256\n s_movk_i32 s42, 2432\n" \
        "s_cmp_lg_u32 %10, 9\n s_cbranch_scc1 2f\n" \
        "s_waitcnt vmcnt(0)\n s_barrier\n" \
        "s_mov_b32 s40, 61440\n s_mov_b32 s41, 73984\n s_mov_b32 s42, 58752\n" \
        "2:\n" \
        KEYTILE("", 1024, "", 128, 608, "", 1216, PL, PH) \
        KEYTILE("v_add_u32 %7, s40, %7\n", 0, "v_add_u32 %8, s41, %8\n", 0, 1824, "v_add_u32 %9, s42, %9\n", 0, PL, PH) \
        "s_sub_u32 %10, %10, 1\n s_cmp_lg_u32 %10, 0\n s_cbranch_scc1 1b\n" \
        "s_waitcnt lgkmcnt(0)\n" \
        "v_smfmac_f32_32x32x32_bf16 %1, v[122:125], v[104:111], %6\n" \
        "s_setprio 0\n s_nop 7\n s_nop 7\n" \
        : "+v"(oA), "+v"(oB), "+v"(Q.fA), "+v"(Q.fB), "+v"(Q.rA), "+v"(Q.rB), "+v"(idx), "+v"(ka), "+v"(k2a), "+v"(va), "+s"(cnt) \
        : "s"(vmask) \
        : "v64", "v65", "v66", "v67", "v68", "v69", "v70", "v71", "v72", "v73", "v74", "v75", "v76", "v77", "v78", "v79", \
          "v80", "v81", "v82", "v83", "v84", "v85", "v86", "v87", "v88", "v89", "v90", "v91", "v92", "v93", "v94", "v95", \
          "v96", "v97", "v98", "v99", "v100", "v101", "v102", "v103", "v104", "v105", "v106", "v107", "v108", "v109", "v110", "v111", \
          "v112", "v113", "v114", "v115", "v116", "v117", "v118", "v119", "v120", "v121", "v122", "v123", "v124", "v125", \
          "s40", "s41", "s42", "scc", "memory")
    if (w < 4) { LOOPASM("s_setprio 0\n", "s_setprio 2\n", "s_setprio 2\n"); } else { LOOPASM("", "", "s_setprio 1\n"); }
#undef LOOPASM
#undef KEYTILE
#undef VRD
#undef EXP4
}
__device__ __forceinline__ void attn_finish(AttnQ& Q, f32x16& oA, f32x16& oB, rsrc_t rs, int head, int qtA, unsigned lds0, int w, int lane) {
    const int kpart = w >> 2, m = lane & 31, h = lane >> 5;
    float mA = 0.f, mB = 0.f;
    if (!attn_range_ok(oA)) attn_slow(lds0 + kpart * CH_B, lane, Q.fA, Q.rA, oA, mA);
    if (!attn_range_ok(oB)) attn_slow(lds0 + kpart * CH_B, lane, Q.fB, Q.rB, oB, mB);
    LBAR();
    {
        LAS float* xch = (LAS float*)(uintptr_t)(lds0 + w * 3072);
        const f32x16& give = kpart ? oA : oB;
#pragma unroll
        for (int r = 0; r < 11; ++r) xch[r * 64 + lane] = give[r];
        xch[11 * 64 + lane] = kpart ? mA : mB;
    }
    LBAR();
    {
        LAS const float* xin = (LAS const float*)(uintptr_t)(lds0 + (w ^ 4) * 3072);
        const f32x16& keep = kpart ? oB : oA;
        const float m0 = kpart ? mB : mA, m1 = xin[11 * 64 + lane];
        const float mm = fmaxf(m0, m1), f0 = __builtin_amdgcn_exp2f(m0 - mm), f1 = __builtin_amdgcn_exp2f(m1 - mm);
        float v[11];
#pragma unroll
        for (int r = 0; r < 11; ++r) v[r] = keep[r] * f0 + xin[r * 64 + lane] * f1;
        const float inv = 1.0f / v[10];
        const int tb = qtA + kpart;
        const u32x4 o8 = {cvtpk(v[0] * inv, v[1] * inv), cvtpk(v[2] * inv, v[3] * inv), cvtpk(v[4] * inv, v[5] * inv), cvtpk(v[6] * inv, v[7] * inv)};
        st16(rs, (unsigned)WS_OM + ((2 * tb + (m >> 4)) * 16 + (head >> 1)) * 1024 + ((2 * (head & 1) + h) * 16 + (m & 15)) * 16, o8);
        const unsigned rr = cvtpk(v[8] * inv, v[9] * inv);
        const unsigned r0 = __shfl(rr, (lane & 7) * 4), r1 = __shfl(rr, (lane & 7) * 4 + 1), r2 = __shfl(rr, (lane & 7) * 4 + 2), r3 = __shfl(rr, (lane & 7) * 4 + 3);
        if (lane < 8) st16(rs, (unsigned)WS_OR + (tb * 32 + head) * 128 + lane * 16, (u32x4){r0, r1, r2, r3});
    }
}

constexpr int P4_SLAB = 0, P4_ZT = 576 * 144, P4_RED = P4_ZT + 72 * 144;
__device__ __forceinline__ void proj_dma(const u16* __restrict__ wo, int cg, unsigned lds0, int w, int lane) {
#pragma unroll
    for (int i = 0; i < 11; ++i) {
        int p = w + 8 * i; p = p > 80 ? 80 : p;
        const int ck = p * 64 + lane, row = ck / 9, c9 = ck - row * 9;
        glds16(wo + (size_t)row * D + cg * 72 + c9 * 8, lds0 + P4_SLAB + p * 1024);
    }
}
struct GateRegs { u32x4 z0, z1, q0, q1; };
__device__ __forceinline__ void gate_issue(GateRegs& R, rsrc_t rs, const u16* __restrict__ bq, int tb, int cg, int w, int lane) {
    const int tid = threadIdx.x;
    R.z0 = ld16(rs, (unsigned)WS_ZT + (unsigned)(cg * 72 + (tid >> 3)) * (S * 2) + (tb * 64 + (tid & 7) * 8) * 2);
    R.z1 = R.z0;
    if (tid < 64) R.z1 = ld16(rs, (unsigned)WS_ZT + (unsigned)(cg * 72 + 64 + (tid >> 3)) * (S * 2) + (tb * 64 + (tid & 7) * 8) * 2);
    const int G = lane >> 4;
    { const int u = 4 * w + G, tg = u / 9, cb = u - tg * 9; R.q0 = *(const u32x4*)(bq + (size_t)(tb * 64 + tg * 16 + (lane & 15)) * D + cg * 72 + cb * 8); }
    R.q1 = R.q0;
    if (w == 0) { const int u = 32 + G, tg = u / 9, cb = u - tg * 9; R.q1 = *(const u32x4*)(bq + (size_t)(tb * 64 + tg * 16 + (lane & 15)) * D + cg * 72 + cb * 8); }
}
__device__ __forceinline__ void gate_stage(const GateRegs& R, unsigned lds0) {
    const int tid = threadIdx.x;
    ldswr4(lds0 + P4_ZT + (tid >> 3) * 144 + (tid & 7) * 16, R.z0);
    if (tid < 64) ldswr4(lds0 + P4_ZT + (64 + (tid >> 3)) * 144 + (tid & 7) * 16, R.z1);
}
__device__ __forceinline__ void gate_unit(unsigned lds0, u16* __restrict__ out, const u32x4& qq, int u, int tb, int cg, int lane) {
    const int tg = u / 9, cb = u - tg * 9, q = (lane & 15) >> 2, p = lane & 3;
    const unsigned a = lds0 + P4_ZT + (8 * cb + q) * 144 + (16 * tg + 4 * p) * 2;
    const u32x2 zl = __builtin_bit_cast(u32x2, trread(a)), zh = __builtin_bit_cast(u32x2, trread(a + 4 * 144));
    const u32x4 r = {cvtpk(bflo(qq[0]) * bflo(zl[0]), bfhi(qq[0]) * bfhi(zl[0])), cvtpk(bflo(qq[1]) * bflo(zl[1]), bfhi(qq[1]) * bfhi(zl[1])),
                     cvtpk(bflo(qq[2]) * bflo(zh[0]), bfhi(qq[2]) * bfhi(zh[0])), cvtpk(bflo(qq[3]) * bflo(zh[1]), bfhi(qq[3]) * bfhi(zh[1]))};
    *(u32x4*)(out + (size_t)(tb * 64 + tg * 16 + (lane & 15)) * OUTW + D + cg * 72 + cb * 8) = r;
}
__device__ __forceinline__ void gate_finish(const GateRegs& R, unsigned lds0, u16* out, int tb, int cg, int w, int lane) {
    gate_unit(lds0, out, R.q0, 4 * w + (lane >> 4), tb, cg, lane);
    if (w == 0) gate_unit(lds0, out, R.q1, 32 + (lane >> 4), tb, cg, lane);
}
__device__ __forceinline__ void proj_tile(rsrc_t rs, u16* __restrict__ out, int tb64, int cg, unsigned lds0, int w, int lane) {
    const int tt = w & 3, kh = w >> 2, t16 = tb64 * 4 + tt;
    bf16x8 af[9];
#pragma unroll
    for (int s = 0; s < 9; ++s) {
        const int st = kh * 9 + s;
        if (st < 16) af[s] = __builtin_bit_cast(bf16x8, ld16(rs, (unsigned)WS_OM + (t16 * 16 + st) * 1024 + lane * 16));
        else {
            const unsigned ro = (unsigned)WS_OR + ((t16 >> 1) * 32 + 16 * (st - 16) + 4 * (lane >> 4)) * 128 + ((t16 & 1) * 16 + (lane & 15)) * 4;
            af[s] = __builtin_bit_cast(bf16x8, (u32x4){ld4(rs, ro), ld4(rs, ro + 128), ld4(rs, ro + 256), ld4(rs, ro + 384)});
        }
    }
    f32x4 acc[5] = {};
    const int kq = lane >> 4, q = (lane & 15) >> 2, p = lane & 3;
#pragma unroll
    for (int s = 0; s < 9; ++s) {
        const int st = kh * 9 + s;
        int r0, r1;
        if (st < 16) { const int hd = 2 * st + (kq >> 1), hh = kq & 1; r0 = 18 * hd + 4 * hh + q; r1 = r0 + 8; }
        else { const int hd = 16 * (st - 16) + 4 * kq + (q >> 1); r0 = 18 * hd + 16 + (q & 1); r1 = r0 + 36; }
#pragma unroll
        for (int c = 0; c < 5; ++c) {
            const int cc = (c == 4 && p >= 2) ? 64 + (p & 1) * 4 : 16 * c + 4 * p;
            const bf16x8 wf = cat8(trread(lds0 + P4_SLAB + r0 * 144 + cc * 2), trread(lds0 + P4_SLAB + r1 * 144 + cc * 2));
            acc[c] = MFMA16(wf, af[s], acc[c]);
        }
    }
    LAS f32x4* red = (LAS f32x4*)(uintptr_t)(lds0 + P4_RED + tt * 5120);
    if (kh == 1) {
#pragma unroll
        for (int c = 0; c < 5; ++c) red[c * 64 + lane] = acc[c];
    }
    LBAR();
    if (kh == 0) {
        u16* op = out + (size_t)(t16 * 16 + (lane & 15)) * OUTW + cg * 72 + 4 * kq;
#pragma unroll
        for (int c = 0; c < 5; ++c) {
            const f32x4 v = acc[c] + red[c * 64 + lane];
            if (c < 4 || kq < 2) *(u32x2*)(op + 16 * c) = (u32x2){cvtpk(v[0], v[1]), cvtpk(v[2], v[3])};
        }
    }
}

#define PROLOG extern __shared__ __attribute__((aligned(16))) char lds[]; const unsigned lds0 = (unsigned)(uintptr_t)lds; \
    const int lane = threadIdx.x & 63, w = __builtin_amdgcn_readfirstlane(threadIdx.x >> 6); const rsrc_t rs = ws_rsrc(ws)

__global__ __launch_bounds__(512, 2) void k1(const u16* __restrict__ x, const u16* __restrict__ wqkv, const u16* __restrict__ bk, const u16* __restrict__ bv,
                                             const u16* __restrict__ shift_k, char* __restrict__ ws) {
    PROLOG;
    STAMP(0, 0);
    phase1(x, wqkv, bk, bv, shift_k, ws, rs, lds0, w, lane);
    STAMP(0, 4);
}
__global__ __launch_bounds__(512, 2) void k2(const u16* __restrict__ ssm_k, const u16* __restrict__ ssm_D, char* __restrict__ ws) {
    PROLOG;
    const int b = blockIdx.x;
    STAMP(1, 0);
    if (b < 224) conv_pair(ssm_k, ssm_D, rs, b, lds0, w, lane);
    else { conv_pair(ssm_k, ssm_D, rs, 224 + 2 * (b - 224), lds0, w, lane); LBAR(); conv_pair(ssm_k, ssm_D, rs, 225 + 2 * (b - 224), lds0, w, lane); }
    STAMP(1, 1);
}
__global__ __launch_bounds__(512, 2) void k3(char* __restrict__ ws) {
    PROLOG;
    const int b = blockIdx.x, head = (b & 7) * 4 + ((b >> 3) & 3), qb = b >> 5, qtA = qb * 8 + (w & 3) * 2;
    STAMP(2, 0);
    AttnQ Q;
    attn_loadq(Q, ws, head, qtA, lane);
    attn_dma(ws, head, 0, lds0, w, lane);
    attn_dma(ws, head, 2, lds0, w, lane);
    ATTN_QWAITBAR(Q, 10);
    attn_fixq(Q, lane);
    STAMP(2, 1);
    f32x16 oA, oB;
    attn_loop(Q, lds0, w, lane, oA, oB);
    STAMP(2, 2);
    attn_finish(Q, oA, oB, rs, head, qtA, lds0, w, lane);
    STAMP(2, 3);
}
__global__ __launch_bounds__(512, 2) void k4(const u16* __restrict__ wo, const u16* __restrict__ bq, char* __restrict__ ws, u16* __restrict__ out) {
    PROLOG;
    const int b = blockIdx.x;
    STAMP(3, 0);
    proj_dma(wo, b & 7, lds0, w, lane);
    GateRegs G;
    gate_issue(G, rs, bq, b >> 3, b & 7, w, lane);
    gate_stage(G, lds0);
    WAITBAR(0);
    STAMP(3, 1);
    gate_finish(G, lds0, out, b >> 3, b & 7, w, lane);
    STAMP(3, 2);
    proj_tile(rs, out, b >> 3, b & 7, lds0, w, lane);
    STAMP(3, 3);
}

extern "C" void kernel_launch(void* const* d_in, const int* in_sizes, int n_in, void* d_out, int out_size, void* d_ws, size_t ws_size,
                              hipStream_t stream) {
    const u16* x = (const u16*)d_in[0];
    const u16* wqkv = (const u16*)d_in[1];
    const u16* wo = (const u16*)d_in[2];
    const u16* bq = (const u16*)d_in[3];
    const u16* bk = (const u16*)d_in[4];
    const u16* bv = (const u16*)d_in[5];
    const u16* shift_k = (const u16*)d_in[6];
    const u16* ssm_k = (const u16*)d_in[7];
    const u16* ssm_D = (const u16*)d_in[8];
    char* ws = (char*)d_ws;
    (void)hipFuncSetAttribute((const void*)k1, hipFuncAttributeMaxDynamicSharedMemorySize, LDS_BYTES);
    (void)hipFuncSetAttribute((const void*)k2, hipFuncAttributeMaxDynamicSharedMemorySize, LDS_BYTES);
    (void)hipFuncSetAttribute((const void*)k3, hipFuncAttributeMaxDynamicSharedMemorySize, LDS_BYTES);
    (void)hipFuncSetAttribute((const void*)k4, hipFuncAttributeMaxDynamicSharedMemorySize, LDS_BYTES);
    k1<<<256, 512, LDS_BYTES, stream>>>(x, wqkv, bk, bv, shift_k, ws);
    k2<<<256, 512, LDS_BYTES, stream>>>(ssm_k, ssm_D, ws);
    k3<<<256, 512, LDS_BYTES, stream>>>(ws);
    k4<<<256, 512, LDS_BYTES, stream>>>(wo, bq, ws, (u16*)d_out);
}
```

```cpp
#include <hip/hip_runtime.h>
#include <hip/hip_bf16.h>
#include <stdint.h>

#define LAS __attribute__((address_space(3)))
typedef __attribute__((ext_vector_type(8))) __bf16 bf16x8;
typedef __attribute__((ext_vector_type(16))) __bf16 bf16x16;
typedef __attribute__((ext_vector_type(4))) short s16x4;
typedef __attribute__((ext_vector_type(8))) short s16x8;
typedef __attribute__((ext_vector_type(16))) float f32x16;
typedef __attribute__((ext_vector_type(8))) float f32x8;
typedef __attribute__((ext_vector_type(4))) float f32x4;
typedef __attribute__((ext_vector_type(4))) unsigned u32x4;
typedef __attribute__((ext_vector_type(2))) unsigned u32x2;
typedef unsigned short u16;

constexpr int S = 2048, D = 576, NH = 32;
constexpr int OUTW = 2 * D;
constexpr int CH_K2 = 16384, CH_VT = 18432, CH_B = 37888, HEAD_B = 4 * CH_B;
constexpr int QT_B = 1152, QH_B = 64 * QT_B;
constexpr size_t WS_FLAG = 0;
constexpr size_t WS_KV = 8192;
constexpr size_t WS_Q = WS_KV + (size_t)NH * HEAD_B;
constexpr size_t WS_OM = WS_Q + (size_t)NH * QH_B;
constexpr size_t WS_OR = WS_OM + (size_t)128 * 16 * 1024;
constexpr size_t WS_KVT = WS_OR + (size_t)64 * 32 * 128;
constexpr size_t WS_ZT = WS_KVT + (size_t)S * D * 2;
constexpr size_t WS_END = WS_ZT + (size_t)S * D * 2;
constexpr float QSCALE = 0.23570226039551584f * 1.4426950408889634f;
constexpr int LDS_BYTES = 158720;

#define STAMP_DECL
#define STAMP(kid, pt)
#define STAMP_FLUSH(kid)
#define STAMP_ARG
#define STAMP_PASS
#define SBAR() __builtin_amdgcn_sched_barrier(0)
#define MFMA32(a, b, c) __builtin_amdgcn_mfma_f32_32x32x16_bf16(a, b, c, 0, 0, 0)
#define MFMA16(a, b, c) __builtin_amdgcn_mfma_f32_16x16x32_bf16(a, b, c, 0, 0, 0)
#define WAITBAR(V) asm volatile("s_waitcnt vmcnt(" #V ") lgkmcnt(0)\n\ts_barrier" ::: "memory")
#define LBAR() asm volatile("s_waitcnt lgkmcnt(0)\n\ts_barrier" ::: "memory")

__device__ __forceinline__ void glds16(const void* g, unsigned lds_base) {
    unsigned sv;
    asm volatile("s_mov_b32 %0, m0\n\ts_mov_b32 m0, %2\n\ts_nop 0\n\tglobal_load_lds_dwordx4 %1, off\n\ts_mov_b32 m0, %0"
                 : "=&s"(sv) : "v"(g), "s"(lds_base) : "memory");
}
__device__ __forceinline__ unsigned cvtpk(float lo, float hi) {
    typedef __attribute__((ext_vector_type(2))) float f2; typedef __attribute__((ext_vector_type(2))) __bf16 b2;
    f2 v = {lo, hi};
    return __builtin_bit_cast(unsigned, __builtin_convertvector(v, b2));
}
__device__ __forceinline__ float bflo(unsigned u) { return __uint_as_float(u << 16); }
__device__ __forceinline__ float bfhi(unsigned u) { return __uint_as_float(u & 0xffff0000u); }
__device__ __forceinline__ float rbf(float x) { return bflo(cvtpk(x, 0.f)); }
__device__ __forceinline__ s16x4 trread(unsigned a) {
    return __builtin_bit_cast(s16x4, __builtin_amdgcn_ds_read_tr16_b64_v4i16((LAS s16x4*)(uintptr_t)a));
}
__device__ __forceinline__ bf16x8 cat8(s16x4 a, s16x4 b) { return __builtin_bit_cast(bf16x8, (s16x8)__builtin_shufflevector(a, b, 0, 1, 2, 3, 4, 5, 6, 7)); }
__device__ __forceinline__ bf16x8 ldsrd128(unsigned a) { return *(LAS const bf16x8*)(uintptr_t)a; }
__device__ __forceinline__ u32x4 ldsrdu4(unsigned a) { return *(LAS const u32x4*)(uintptr_t)a; }
__device__ __forceinline__ u32x2 ldsrdu2(unsigned a) { return *(LAS const u32x2*)(uintptr_t)a; }
__device__ __forceinline__ void ldswr4(unsigned a, u32x4 v) { *(LAS u32x4*)(uintptr_t)a = v; }
__device__ __forceinline__ void ldswr2(unsigned a, u32x2 v) { *(LAS u32x2*)(uintptr_t)a = v; }
__device__ __forceinline__ void ldswr1(unsigned a, unsigned v) { *(LAS unsigned*)(uintptr_t)a = v; }

__device__ __forceinline__ unsigned l2touch(const void* p) { unsigned r; asm volatile("global_load_dword %0, %1, off" : "=v"(r) : "v"(p) : "memory"); return r; }
#define KEEP(x) asm volatile("" :: "v"(x))

typedef __amdgpu_buffer_rsrc_t rsrc_t;
__device__ __forceinline__ rsrc_t ws_rsrc(char* ws) { return __builtin_amdgcn_make_buffer_rsrc(ws, 0, (int)WS_END, 0x00020000); }
__device__ __forceinline__ void st16(rsrc_t r, unsigned off, u32x4 v) { __builtin_amdgcn_raw_buffer_store_b128(v, r, off, 0, 16); }
__device__ __forceinline__ void st8(rsrc_t r, unsigned off, u32x2 v) { __builtin_amdgcn_raw_buffer_store_b64(v, r, off, 0, 16); }
__device__ __forceinline__ void st4(rsrc_t r, unsigned off, unsigned v) { __builtin_amdgcn_raw_buffer_store_b32(v, r, off, 0, 16); }
__device__ __forceinline__ u32x4 ld16(rsrc_t r, unsigned off) { return __builtin_amdgcn_raw_buffer_load_b128(r, off, 0, 16); }
__device__ __forceinline__ unsigned ld4(rsrc_t r, unsigned off) { return __builtin_amdgcn_raw_buffer_load_b32(r, off, 0, 16); }

constexpr int KVS_K = 0, KVS_W = 67 * 144, KVS_T = KVS_W + 512, KVS_B = KVS_T + 64 * 144;
struct KvRegs { u32x4 k, v, x; };
__device__ __forceinline__ u32x4 gload16(const void* p) {
    u32x4 r; asm volatile("global_load_dwordx4 %0, %1, off" : "=v"(r) : "v"(p) : "memory"); return r;
}
__device__ __forceinline__ void kv_issue(KvRegs& R, const u16* __restrict__ bk, const u16* __restrict__ bv, const u16* __restrict__ shift_k, int tb, int cg) {
    const int tid = threadIdx.x, t = tb * 64 + (tid >> 3), c = cg * 64 + (tid & 7) * 8;
    R.k = gload16(bk + (size_t)t * D + c);
    R.v = gload16(bv + (size_t)t * D + c);
    const u16* xp = bk + c;
    if (tid < 24) { const int th = tb * 64 - 3 + (tid >> 3); xp = bk + (size_t)(th < 0 ? 0 : th) * D + c; }
    else if (tid >= 64 && tid < 96) xp = shift_k + cg * 256 + (tid - 64) * 8;
    R.x = gload16(xp);
}
#define KV_WAIT(R, N) asm volatile("s_waitcnt vmcnt(" #N ")" : "+v"(R.k), "+v"(R.v), "+v"(R.x) :: "memory")
__device__ __forceinline__ void kv_stage(const KvRegs& R, int tb, unsigned sc) {
    const int tid = threadIdx.x;
    ldswr4(sc + KVS_K + ((tid >> 3) + 3) * 144 + (tid & 7) * 16, R.k);
    if (tid < 24) ldswr4(sc + KVS_K + (tid >> 3) * 144 + (tid & 7) * 16, (tb * 64 - 3 + (tid >> 3) < 0) ? (u32x4){0u, 0u, 0u, 0u} : R.x);
    else if (tid >= 64 && tid < 96) ldswr4(sc + KVS_W + (tid - 64) * 16, R.x);
}
__device__ __forceinline__ float dot2bf(unsigned a, unsigned b, float c) {
    typedef __attribute__((ext_vector_type(2))) __bf16 b2;
    return __builtin_amdgcn_fdot2_f32_bf16(__builtin_bit_cast(b2, a), __builtin_bit_cast(b2, b), c, false);
}
__device__ __forceinline__ unsigned kv_pair(unsigned k0, unsigned k1, unsigned k2, unsigned k3, unsigned vv, unsigned wa0, unsigned wa1, unsigned wb0, unsigned wb1) {
    const float sl = dot2bf(__builtin_amdgcn_perm(k2, k3, 0x05040100u), wa0, dot2bf(__builtin_amdgcn_perm(k0, k1, 0x05040100u), wa1, 0.f));
    const float sh = dot2bf(__builtin_amdgcn_perm(k2, k3, 0x07060302u), wb0, dot2bf(__builtin_amdgcn_perm(k0, k1, 0x07060302u), wb1, 0.f));
    const unsigned s = cvtpk(sl, sh);
    return cvtpk(bflo(s) * bflo(vv), bfhi(s) * bfhi(vv));
}
__device__ __forceinline__ void kv_compute(const KvRegs& R, unsigned sc) {
    const int tid = threadIdx.x, trow = tid >> 3, ch = tid & 7;
    const unsigned ka = sc + KVS_K + trow * 144 + ch * 16;
    const u32x4 k0 = ldsrdu4(ka), k1 = ldsrdu4(ka + 144), k2 = ldsrdu4(ka + 288);
    const unsigned wa = sc + KVS_W + ch * 64;
    const u32x4 w0 = ldsrdu4(wa), w1 = ldsrdu4(wa + 16), w2 = ldsrdu4(wa + 32), w3 = ldsrdu4(wa + 48);
    const u32x4 r = {kv_pair(k0[0], k1[0], k2[0], R.k[0], R.v[0], w0[0], w0[1], w0[2], w0[3]),
                     kv_pair(k0[1], k1[1], k2[1], R.k[1], R.v[1], w1[0], w1[1], w1[2], w1[3]),
                     kv_pair(k0[2], k1[2], k2[2], R.k[2], R.v[2], w2[0], w2[1], w2[2], w2[3]),
                     kv_pair(k0[3], k1[3], k2[3], R.k[3], R.v[3], w3[0], w3[1], w3[2], w3[3])};
    const int row = (trow & ~7) | (7 - (trow & 7));
    ldswr4(sc + KVS_T + row * 144 + ch * 16, r);
}
__device__ __forceinline__ void kv_store(rsrc_t rs, int tb, int cg, unsigned sc, int w, int lane) {
    const int G = lane >> 4, q = (lane & 15) >> 2, p = lane & 3;
    const int cw = (w >> 1) * 16;
    s16x4 a[4];
#pragma unroll
    for (int u = 0; u < 4; ++u) a[u] = trread(sc + KVS_T + (16 * G + 4 * u + q) * 144 + (cw + 4 * p) * 2);
    const int i = lane & 15;
    if ((i >> 3) == (w & 1)) {
        const unsigned off = (unsigned)WS_KVT + (unsigned)(cg * 64 + cw + i) * (S * 2) + (tb * 64 + 16 * G) * 2;
        st16(rs, off, __builtin_bit_cast(u32x4, (s16x8)__builtin_shufflevector(a[0], a[1], 0, 1, 2, 3, 4, 5, 6, 7)));
        st16(rs, off + 16, __builtin_bit_cast(u32x4, (s16x8)__builtin_shufflevector(a[2], a[3], 0, 1, 2, 3, 4, 5, 6, 7)));
    }
}

#define NOKV 0
#define K1PF 0
constexpr int G1_STAGE = 32768, G1_SC = 4 * G1_STAGE;

template <bool ISV>
__device__ __forceinline__ void qkv_tile(const u16* __restrict__ x, const u16* __restrict__ wqkv, char* __restrict__ ws, rsrc_t rs,
                                         int mb, int ng, unsigned lds0, int w, int lane,
                                         const u16* __restrict__ bk, const u16* __restrict__ bv, const u16* __restrict__ shift_k, int kvtb, int kvcg STAMP_ARG) {
    const int ctl = w & 3, tb0 = (w >> 2) * 2;
    const bool active = (ng * 4 + ctl) < 54;
    const u16* xsrc[2]; const u16* wsrc[2];
#pragma unroll
    for (int i = 0; i < 2; ++i) {
        const int p = w * 2 + i;
        const int xr = 8 * p + (lane >> 3), xc = (lane & 7) ^ ((xr >> 1) & 7);
        xsrc[i] = x + (size_t)(mb * 128 + xr) * D + xc * 8;
        const int wr = 4 * p + (lane >> 4), wc = (lane & 15) ^ (((wr & 3) << 2) | ((wr >> 2) & 3));
        int col = ng * 128 + wc * 8; col = col > 1720 ? 1720 : col;
        wsrc[i] = wqkv + (size_t)wr * (3 * D) + col;
    }
    auto issue1 = [&](int c, int pc) {
        const unsigned st = lds0 + (c & 3) * G1_STAGE;
        const int i = pc >> 1;
        if (pc & 1) glds16(wsrc[i] + (size_t)c * 64 * (3 * D), st + 16384 + (w * 2 + i) * 1024);
        else glds16(xsrc[i] + c * 64, st + (w * 2 + i) * 1024);
    };
    auto issue = [&](int c) { issue1(c, 0); issue1(c, 1); issue1(c, 2); issue1(c, 3); };
    const int r = lane & 31, h = lane >> 5;
    unsigned xa[4];
#pragma unroll
    for (int s = 0; s < 4; ++s) xa[s] = lds0 + (tb0 * 32 + r) * 128 + (((2 * s + h) ^ ((r >> 1) & 7)) << 4);
    unsigned wa[2];
    {   const int blk = (lane >> 4) & 1, q = (lane & 15) >> 2, p = lane & 3;
#pragma unroll
        for (int u = 0; u < 2; ++u) {
            const int row = 8 * h + 4 * u + q, ch = 4 * ctl + 2 * blk + (p >> 1);
            wa[u] = lds0 + 16384 + row * 256 + ((ch ^ (((row & 3) << 2) | ((row >> 2) & 3))) << 4) + 8 * (p & 1);
        } }
    f32x16 acc0 = {}, acc1 = {};
    KvRegs R;
    unsigned pf = 0;
    if (K1PF) {
        const int b = blockIdx.x, xcd = b & 7, li = (b >> 3) * 396 + (int)threadIdx.x;
        if (threadIdx.x < 396) {
            const u16* p;
            if (li < 4608) p = x + (size_t)((xcd >> 1) * 512 + li / 9) * D + (li % 9) * 64;
            else { const int l2 = li - 4608; int col = (xcd & 1) * 896 + (l2 % 14) * 64; col = col > 1664 ? 1664 : col; p = wqkv + (size_t)(l2 / 14) * (3 * D) + col; }
            pf = l2touch(p);
        }
    }
    issue(0);
    if (NOKV == 0) kv_issue(R, bk, bv, shift_k, kvtb, kvcg); else { R.k = R.v = R.x = (u32x4){0u,0u,0u,0u}; }
    issue(1);
    STAMP(0, 1);
    struct Frags { bf16x8 w[4], x0[4], x1[4]; };
    Frags fa, fb;
    auto rd = [&](Frags& f, int c, int s) {
        const unsigned st = (c & 3) * G1_STAGE;
        f.w[s] = cat8(trread(wa[0] + st + s * 4096), trread(wa[1] + st + s * 4096));
        f.x0[s] = ldsrd128(xa[s] + st); f.x1[s] = ldsrd128(xa[s] + st + 4096);
    };
    auto mm = [&](const Frags& f, int s) {
        if (ISV) { acc0 = MFMA32(f.x0[s], f.w[s], acc0); acc1 = MFMA32(f.x1[s], f.w[s], acc1); }
        else     { acc0 = MFMA32(f.w[s], f.x0[s], acc0); acc1 = MFMA32(f.w[s], f.x1[s], acc1); }
    };
#define G1_STEP(CUR, NXT, c, N, I0, I1) do { WAITBAR(N); SBAR(); if ((I0) < 9) issue(I0); if ((I1) < 9) issue(I1); SBAR(); \
        if (active) { _Pragma("unroll") for (int s = 0; s < 4; ++s) { rd(NXT, (c) + 1, s); SBAR(); mm(CUR, s); SBAR(); } } } while (0)
    const unsigned sc = lds0 + G1_SC;
    KV_WAIT(R, 4);
    kv_stage(R, kvtb, sc);
    WAITBAR(4);
    STAMP(0, 2);
    if (active) { _Pragma("unroll") for (int s = 0; s < 4; ++s) rd(fa, 0, s); }
    issue(2);
    if (NOKV == 0) kv_compute(R, sc);
    STAMP(0, 3);
    G1_STEP(fa, fb, 0, 4, 3, 4);
    STAMP(0, 4);
    STAMP(0, 5);
    G1_STEP(fb, fa, 1, 8, 5, 9);
    G1_STEP(fa, fb, 2, 8, 6, 9);
    G1_STEP(fb, fa, 3, 8, 7, 9);
    G1_STEP(fa, fb, 4, 8, 8, 9);
    G1_STEP(fb, fa, 5, 8, 9, 9);
    G1_STEP(fa, fb, 6, 4, 9, 9);
    G1_STEP(fb, fa, 7, 0, 9, 9);
    if (active) { _Pragma("unroll") for (int s = 0; s < 4; ++s) mm(fa, s); }
    KEEP(pf);
    STAMP(0, 6);
    kv_store(rs, kvtb, kvcg, sc, w, lane);
    const int ct = ng * 4 + ctl;
    if (ISV) {
        if (active) {
#pragma unroll
            for (int i = 0; i < 2; ++i) {
                const f32x16& acc = i ? acc1 : acc0;
                const int T = mb * 4 + tb0 + i;
                const int col = (ct - 36) * 32 + r, head = col / 18, d = col - head * 18;
                const unsigned vp = (unsigned)WS_KV + head * HEAD_B + (((T >> 4) & 1) * 2 + (T >> 5)) * CH_B + CH_VT + (2 * (T & 15)) * 608 + h * 304 + d * 16;
#pragma unroll
                for (int g = 0; g < 2; ++g) {
                    const u32x4 o = {cvtpk(acc[8 * g], acc[8 * g + 1]), cvtpk(acc[8 * g + 2], acc[8 * g + 3]),
                                     cvtpk(acc[8 * g + 4], acc[8 * g + 5]), cvtpk(acc[8 * g + 6], acc[8 * g + 7])};
                    st16(rs, vp + g * 608, o);
                    if (d == 17) st16(rs, vp + g * 608 + 16, (u32x4){0x3f803f80u, 0x3f803f80u, 0x3f803f80u, 0x3f803f80u});
                }
            }
        }
    } else {
        LBAR();
        if (active) {
            const bool isq = ct < 18;
            const float scl = isq ? QSCALE : 1.0f;
            const int row = isq ? r : ((r & 16) | ((r & 4) << 1) | ((r & 8) >> 1) | (r & 3));
#pragma unroll
            for (int i = 0; i < 2; ++i) {
                const f32x16& acc = i ? acc1 : acc0;
#pragma unroll
                for (int g = 0; g < 4; ++g)
                    ldswr2(lds0 + (tb0 + i) * 8192 + (8 * ctl + 2 * g + h) * 256 + row * 8,
                           (u32x2){cvtpk(acc[4 * g] * scl, acc[4 * g + 1] * scl), cvtpk(acc[4 * g + 2] * scl, acc[4 * g + 3] * scl)});
            }
        }
        LBAR();
        const int g0 = ng * 32;
#pragma unroll
        for (int k5 = 0; k5 < 5; ++k5) {
            const int it = w + 8 * k5, tt = it / 10, cand = (it % 10) * 2 + h;
            const int blk0 = g0 / 9, blk = blk0 + (cand >> 2), cpos = cand & 3;
            const int pos = (cpos & 1) * 2 + (cpos >> 1) * 5;
            const int gq = blk * 9 + pos, lq = gq - g0;
            const bool in0 = lq >= 0 && lq < 32 && gq < 288, in1 = lq + 1 >= 0 && lq + 1 < 32 && gq + 1 < 288;
            if (in0 || in1) {
                const bool isq = gq < 144;
                const int pb = isq ? blk : blk - 16, head = 2 * pb + (cpos >> 1), hp = cpos & 1;
                const int T = mb * 4 + tt;
                const unsigned dst = isq ? (unsigned)WS_Q + head * QH_B + T * QT_B + hp * 512 + r * 16
                                         : (unsigned)WS_KV + head * HEAD_B + (((T >> 4) & 1) * 2 + (T >> 5)) * CH_B + (T & 15) * 1024 + hp * 512 + r * 16;
                const unsigned sa = lds0 + tt * 8192 + lq * 256 + r * 8;
                if (in0 && in1) { const u32x2 a = ldsrdu2(sa), b = ldsrdu2(sa + 256); st16(rs, dst, (u32x4){a[0], a[1], b[0], b[1]}); }
                else if (in0) st8(rs, dst, ldsrdu2(sa));
                else st8(rs, dst + 8, ldsrdu2(sa + 256));
            }
        }
        {
            const int blk = g0 / 9 + w, gq = blk * 9 + 4, lq = gq - g0;
            if (w < 5 && lq >= 0 && lq < 32 && gq < 288) {
                const int tt = lane >> 4, par = (lane >> 3) & 1, rq = lane & 7;
                const bool isq = gq < 144;
                const int head = 2 * (isq ? blk : blk - 16) + par, T = mb * 4 + tt;
                const unsigned sa = lds0 + tt * 8192 + lq * 256 + rq * 32 + par * 4;
                const u32x4 v = {*(LAS const unsigned*)(uintptr_t)sa, *(LAS const unsigned*)(uintptr_t)(sa + 8),
                                 *(LAS const unsigned*)(uintptr_t)(sa + 16), *(LAS const unsigned*)(uintptr_t)(sa + 24)};
                const unsigned dst = isq ? (unsigned)WS_Q + head * QH_B + T * QT_B + 1024 + rq * 16
                                         : (unsigned)WS_KV + head * HEAD_B + (((T >> 4) & 1) * 2 + (T >> 5)) * CH_B + CH_K2 + (T & 15) * 128 + rq * 16;
                st16(rs, dst, v);
            }
        }
    }
}

__device__ __forceinline__ void phase1(const u16* x, const u16* wqkv, const u16* bk, const u16* bv, const u16* shift_k, char* ws, rsrc_t rs,
                                       unsigned lds0, int w, int lane STAMP_ARG) {
    const int b = blockIdx.x;
    if (NOKV == 2 && b >= 224) return;
    if (b >= 224) {
        const int t = 224 + (b - 224) * 2;
        unsigned pf = 0;
        if (K1PF) {
            const int xcd = b & 7, li = (b >> 3) * 396 + (int)threadIdx.x;
            if (threadIdx.x < 396) {
                const int l2 = li - 4608; int col = (xcd & 1) * 896 + (l2 % 14) * 64; col = col > 1664 ? 1664 : col;
                pf = l2touch(wqkv + (size_t)(l2 / 14) * (3 * D) + col);
            }
        }
        KvRegs R0, R1;
        kv_issue(R0, bk, bv, shift_k, t & 31, t >> 5); kv_issue(R1, bk, bv, shift_k, (t + 1) & 31, (t + 1) >> 5);
        KV_WAIT(R0, 3); kv_stage(R0, t & 31, lds0);
        KV_WAIT(R1, 0); kv_stage(R1, (t + 1) & 31, lds0 + KVS_B);
        LBAR();
        kv_compute(R0, lds0); kv_compute(R1, lds0 + KVS_B);
        LBAR();
        kv_store(rs, t & 31, t >> 5, lds0, w, lane); kv_store(rs, (t + 1) & 31, (t + 1) >> 5, lds0 + KVS_B, w, lane);
        KEEP(pf);
        return;
    }
    const int xcd = b & 7, i = b >> 3, mb = (xcd >> 1) * 4 + (i & 3), ng = (xcd & 1) * 7 + (i >> 2);
    if (ng >= 9) qkv_tile<true>(x, wqkv, ws, rs, mb, ng, lds0, w, lane, bk, bv, shift_k, b & 31, b >> 5 STAMP_PASS);
    else qkv_tile<false>(x, wqkv, ws, rs, mb, ng, lds0, w, lane, bk, bv, shift_k, b & 31, b >> 5 STAMP_PASS);
}

constexpr int CV_U = 33280, CV_B = CV_U + 7600;
constexpr int CV_BASE = LDS_BYTES - 2 * CV_B;

struct ConvRegs { u32x4 h0, h1; unsigned h2; u32x4 u; unsigned e[8]; };
__device__ __forceinline__ void conv_issue(ConvRegs& R, const u16* __restrict__ ssm_k, rsrc_t rs, int c) {
    const int i = threadIdx.x & 255;
    const rsrc_t hr = __builtin_amdgcn_make_buffer_rsrc((void*)(ssm_k + (size_t)c * S), 0, S * 2, 0x00020000);
    const unsigned off = i < 4 ? 0u : (unsigned)(((9 * i - 32) >> 1) * 4);
    if (i < 64) {
#pragma unroll
        for (int j = 0; j < 8; ++j) R.e[j] = __builtin_amdgcn_raw_buffer_load_b16(hr, (unsigned)((i - 32 + j) * 2), 0, 0);
    }
    R.h0 = __builtin_amdgcn_raw_buffer_load_b128(hr, off, 0, 0);
    R.h1 = __builtin_amdgcn_raw_buffer_load_b128(hr, off + 16, 0, 0);
    R.h2 = __builtin_amdgcn_raw_buffer_load_b32(hr, off + 32, 0, 0);
    R.u = ld16(rs, (unsigned)WS_KVT + (unsigned)c * (S * 2) + i * 16);
}
__device__ __forceinline__ void conv_build(const ConvRegs& R, unsigned base) {
    const int i = threadIdx.x & 255;
    if (i < 36) ldswr4(base + i * 16, (u32x4){R.e[0] | (R.e[1] << 16), R.e[2] | (R.e[3] << 16), R.e[4] | (R.e[5] << 16), R.e[6] | (R.e[7] << 16)});
    if (i >= 4 && i < 232) {
        const unsigned sh = (i & 1) * 16;
        const unsigned dw[9] = {R.h0[0], R.h0[1], R.h0[2], R.h0[3], R.h1[0], R.h1[1], R.h1[2], R.h1[3], R.h2};
        unsigned A[8], Bq[7];
#pragma unroll
        for (int n = 0; n < 8; ++n) A[n] = __builtin_amdgcn_alignbit(dw[n + 1], dw[n], sh);
#pragma unroll
        for (int n = 0; n < 7; ++n) Bq[n] = __builtin_amdgcn_alignbit(A[n + 1], A[n], 16);
        const unsigned a = base + 9 * i * 16;
#pragma unroll
        for (int u = 0; u < 9; ++u) {
            if (i < 231 || u < 1) {
                if (u & 1) ldswr4(a + u * 16, (u32x4){Bq[u >> 1], Bq[(u >> 1) + 1], Bq[(u >> 1) + 2], Bq[(u >> 1) + 3]});
                else ldswr4(a + u * 16, (u32x4){A[u >> 1], A[(u >> 1) + 1], A[(u >> 1) + 2], A[(u >> 1) + 3]});
            }
        }
    }
    ldswr4(base + CV_U + (31 + (i >> 2)) * 80 + (i & 3) * 16, R.u);
    if (i < 155) ldswr4(base + CV_U + i * 16, (u32x4){0u, 0u, 0u, 0u});
}
template <int JB, int JE>
__device__ __forceinline__ void conv_range(unsigned aaddr, unsigned baddr, f32x16& d0, f32x16& d1) {
    auto boff = [](int j) { const int c2 = 1040 - 16 * j; return ((c2 >> 5) + 31) * 80 + (c2 & 16) * 2; };
    constexpr int PD = 4;
    bf16x8 a[PD], b1[PD], b0[PD];
#pragma unroll
    for (int j = JB; j < JB + PD; ++j) { a[(j - JB) % PD] = ldsrd128(aaddr + j * 256); b1[(j - JB) % PD] = ldsrd128(baddr + boff(j)); if (j < 64) b0[(j - JB) % PD] = ldsrd128(baddr + boff(j) - 2560); }
    SBAR();
#pragma unroll
    for (int j = JB; j < JE; ++j) {
        const int k = (j - JB) % PD;
        d1 = MFMA32(a[k], b1[k], d1);
        if (j < 64) d0 = MFMA32(a[k], b0[k], d0);
        SBAR();
        if (j + PD < JE) { a[k] = ldsrd128(aaddr + (j + PD) * 256); b1[k] = ldsrd128(baddr + boff(j + PD)); if (j + PD < 64) b0[k] = ldsrd128(baddr + boff(j + PD) - 2560); }
        SBAR();
    }
}
__device__ __forceinline__ void conv_main(unsigned base, int wg, int lane, f32x16& d0, f32x16& d1) {
    const int n = lane & 31, hh = lane >> 5;
    const unsigned aaddr = base + (n - 8 * hh + 9) * 16, baddr = base + CV_U + n * 80 + hh * 16;
    d0 = f32x16{}; d1 = f32x16{};
    if (wg == 0) conv_range<0, 24>(aaddr, baddr, d0, d1);
    else if (wg == 1) conv_range<24, 48>(aaddr, baddr, d0, d1);
    else if (wg == 2) conv_range<48, 80>(aaddr, baddr, d0, d1);
    else conv_range<80, 128>(aaddr, baddr, d0, d1);
}
__device__ __forceinline__ void conv_partials(unsigned base, int wg, int lane, const f32x16& d0, const f32x16& d1) {
    const int n = lane & 31, hh = lane >> 5;
    const unsigned p = base + wg * 9216 + (36 * n + 4 * hh) * 4;
#pragma unroll
    for (int g = 0; g < 4; ++g) {
        if (wg < 3) *(LAS f32x4*)(uintptr_t)(p + g * 32) = (f32x4){d0[4 * g], d0[4 * g + 1], d0[4 * g + 2], d0[4 * g + 3]};
        *(LAS f32x4*)(uintptr_t)(p + 4608 + g * 32) = (f32x4){d1[4 * g], d1[4 * g + 1], d1[4 * g + 2], d1[4 * g + 3]};
    }
}
__device__ __forceinline__ void conv_finish(unsigned base, rsrc_t rs, const u16* __restrict__ ssm_D, int c, const u32x4& uu) {
    const int i = threadIdx.x & 255;
    const unsigned p = base + (8 * i + 4 * (i >> 2)) * 4;
    f32x4 lo = *(LAS const f32x4*)(uintptr_t)p, hi = *(LAS const f32x4*)(uintptr_t)(p + 16);
#pragma unroll
    for (int k = 1; k < 4; ++k)
        if (k < 3 || i >= 128) { lo += *(LAS const f32x4*)(uintptr_t)(p + k * 9216); hi += *(LAS const f32x4*)(uintptr_t)(p + k * 9216 + 16); }
    const float dd = bflo(ssm_D[c]);
    const float y[8] = {lo[0], lo[1], lo[2], lo[3], hi[0], hi[1], hi[2], hi[3]};
    unsigned o[4];
#pragma unroll
    for (int j = 0; j < 4; ++j) {
        const float u0 = bfhi(uu[3 - j]), u1 = bflo(uu[3 - j]);
        o[j] = cvtpk(rbf(y[2 * j]) + rbf(u0 * dd), rbf(y[2 * j + 1]) + rbf(u1 * dd));
    }
    st16(rs, (unsigned)WS_ZT + (unsigned)c * (S * 2) + i * 16, (u32x4){o[0], o[1], o[2], o[3]});
}
__device__ __forceinline__ void conv_pair(const u16* ssm_k, const u16* ssm_D, char* ws, rsrc_t rs, int pair, unsigned lds0, int w, int lane STAMP_ARG) {
    const int grp = w >> 2, c = 2 * pair + grp;
    const unsigned base = lds0 + CV_BASE + grp * CV_B;
    ConvRegs R;
    conv_issue(R, ssm_k, rs, c);
    conv_build(R, base);
    STAMP(1, 1);
    LBAR();
    f32x16 d0, d1;
    conv_main(base, w & 3, lane, d0, d1);
    STAMP(1, 2);
    LBAR();
    conv_partials(base, w & 3, lane, d0, d1);
    LBAR();
    conv_finish(base, rs, ssm_D, c, R.u);
    STAMP(1, 3);
}

__device__ __forceinline__ unsigned attn_tile_off(int t) { return t < 16 ? 0u : 2u * CH_B; }
__device__ __forceinline__ void attn_slow(unsigned base, int lane, bf16x8 qf, s16x4 q2, f32x16& o, float& mrow) {
    const int m_ = lane & 31, h = lane >> 5, vrow = (m_ < 19) ? m_ : ((m_ == 22) ? 18 : 0);
    float m = -3.0e38f;
    o = f32x16{};
    for (int t = 0; t < 32; ++t) {
        const unsigned cb = base + attn_tile_off(t), tl = t & 15;
        const bf16x8 kf = ldsrd128(cb + tl * 1024 + lane * 16);
        const unsigned k2 = *(LAS const unsigned*)(uintptr_t)(cb + CH_K2 + tl * 128 + m_ * 4);
        f32x16 s = {};
        s = MFMA32(kf, qf, s);
        s = __builtin_amdgcn_mfma_f32_32x32x8bf16_1k(__builtin_bit_cast(s16x4, (u32x2){k2, 0u}), q2, s, 0, 0, 0);
        float tm = s[0];
#pragma unroll
        for (int r = 1; r < 16; ++r) tm = fmaxf(tm, s[r]);
        tm = fmaxf(tm, __shfl_xor(tm, 32));
        const float mn = fmaxf(m, tm);
        const float f = __builtin_amdgcn_exp2f(m - mn);
        m = mn;
#pragma unroll
        for (int r = 0; r < 16; ++r) { o[r] *= f; s[r] = __builtin_amdgcn_exp2f(s[r] - mn); }
        const f32x8 lo = __builtin_shufflevector(s, s, 0, 1, 2, 3, 4, 5, 6, 7), hi = __builtin_shufflevector(s, s, 8, 9, 10, 11, 12, 13, 14, 15);
        const unsigned va = cb + CH_VT + tl * 1216 + h * 304 + vrow * 16;
        o = __builtin_amdgcn_smfmac_f32_32x32x32_bf16(ldsrd128(va), __builtin_bit_cast(bf16x16, lo), o, (int)0xDDDDDDDD, 0, 0);
        o = __builtin_amdgcn_smfmac_f32_32x32x32_bf16(ldsrd128(va + 608), __builtin_bit_cast(bf16x16, hi), o, (int)0xDDDDDDDD, 0, 0);
    }
    mrow = m;
}
__device__ __forceinline__ bool attn_range_ok(const f32x16& o) {
    const float l = o[10];
    bool ok = (l >= 5.9604645e-8f) && (l <= 1.8446744e19f);
#pragma unroll
    for (int r = 0; r < 10; ++r) ok = ok && (fabsf(o[r]) <= 3.0e38f);
    return __all(ok);
}
__device__ __forceinline__ void attn_dma(const char* ws, int head, int first, unsigned lds0, int w, int lane) {
    const char* src = ws + WS_KV + (size_t)head * HEAD_B + first * CH_B + lane * 16;
#pragma unroll
    for (int i = 0; i < 10; ++i) { int p = w + 8 * i; p = p > 73 ? 73 : p; glds16(src + p * 1024, lds0 + first * CH_B + p * 1024); }
}
struct AttnQ { bf16x8 fA, fB; s16x4 rA, rB; unsigned a, b; };
__device__ __forceinline__ void attn_loadq(AttnQ& Q, const char* ws, int head, int qtA, int lane) {
    const char* qp = ws + WS_Q + (size_t)head * QH_B + (size_t)qtA * QT_B;
    asm volatile("global_load_dwordx4 %0, %1, off sc1" : "=v"(Q.fA) : "v"(qp + lane * 16) : "memory");
    asm volatile("global_load_dwordx4 %0, %1, off sc1" : "=v"(Q.fB) : "v"(qp + QT_B + lane * 16) : "memory");
    asm volatile("global_load_dword %0, %1, off sc1" : "=v"(Q.a) : "v"(qp + 1024 + (lane & 31) * 4) : "memory");
    asm volatile("global_load_dword %0, %1, off sc1" : "=v"(Q.b) : "v"(qp + QT_B + 1024 + (lane & 31) * 4) : "memory");
}
#define ATTN_QWAITBAR(Q, N) asm volatile("s_waitcnt vmcnt(" #N ") lgkmcnt(0)\n\ts_barrier" : "+v"(Q.fA), "+v"(Q.fB), "+v"(Q.a), "+v"(Q.b) :: "memory")
__device__ __forceinline__ void attn_fixq(AttnQ& Q, int lane) {
    Q.rA = __builtin_bit_cast(s16x4, (u32x2){lane < 32 ? Q.a : 0u, 0u}); Q.rB = __builtin_bit_cast(s16x4, (u32x2){lane < 32 ? Q.b : 0u, 0u});
}

__device__ __forceinline__ void attn_loop(AttnQ& Q, const char* ws, int head, unsigned lds0, int w, int lane, f32x16& oA, f32x16& oB) {
    const int kpart = w >> 2, m = lane & 31, h = lane >> 5;
    const int vrow = (m < 19) ? m : ((m == 22) ? 18 : 0);
    unsigned ka = lds0 + kpart * CH_B + lane * 16, k2a = lds0 + kpart * CH_B + CH_K2 + m * 4, va = lds0 + kpart * CH_B + CH_VT + h * 304 + vrow * 16;
    unsigned idx = 0xDDDDDDDDu;
    int cnt = 16;
    const unsigned long long vmask = (((1ull << 19) - 1) | (1ull << 22)) * 0x100000001ull;
    oA = f32x16{}; oB = f32x16{};
    const char* src2 = ws + WS_KV + (size_t)head * HEAD_B + 2 * CH_B;
    unsigned lane16 = lane * 16, ldsb = lds0 + 2 * CH_B, pnext = w;
#define EXP4(d0, d1, d2, d3, s0, s1, s2, s3) \
        "v_exp_f32 v" #d0 ", v" #s0 "\n v_exp_f32 v" #d1 ", v" #s1 "\n v_exp_f32 v" #d2 ", v" #s2 "\n v_exp_f32 v" #d3 ", v" #s3 "\n"
#define VRD(x) "s_mov_b64 exec, %12\n" x "s_mov_b64 exec, -1\n"
#define DMA2 "s_min_u32 s44, %11, 73\n s_lshl_b32 s44, s44, 10\n s_add_u32 m0, %14, s44\n v_add_u32 v126, s44, %13\n s_add_u32 %11, %11, 8\n global_load_lds_dwordx4 v126, %15\n"
#define KEYTILE(PK, OK, PK2, OK2, OVH, PV, OVL, PL, PH) \
        "v_mfma_f32_32x32x16_bf16 v[80:95], v[112:115], %3, 0\n" \
        PK "ds_read_b128 v[112:115], %7 offset:" #OK "\n" \
        PL EXP4(96, 97, 98, 99, 64, 65, 66, 67) PH \
        "v_mfma_f32_32x32x8_bf16 v[80:95], v[116:117], %5, v[80:95]\n" \
        PK2 "ds_read_b32 v116, %8 offset:" #OK2 "\n" \
        EXP4(100, 101, 102, 103, 68, 69, 70, 71) \
        "v_smfmac_f32_32x32x32_bf16 %1, v[122:125], v[104:111], %6\n" \
        VRD("ds_read_b128 v[122:125], %9 offset:" #OVH "\n") \
        EXP4(104, 105, 106, 107, 72, 73, 74, 75) \
        "s_waitcnt lgkmcnt(3)\n" \
        "v_smfmac_f32_32x32x32_bf16 %0, v[118:121], v[96:103], %6\n" \
        EXP4(108, 109, 110, 111, 76, 77, 78, 79) \
        "s_waitcnt lgkmcnt(2)\n" \
        "v_mfma_f32_32x32x16_bf16 v[64:79], v[112:115], %2, 0\n" \
        PL EXP4(96, 97, 98, 99, 80, 81, 82, 83) PH \
        "s_waitcnt lgkmcnt(1)\n" \
        "v_mfma_f32_32x32x8_bf16 v[64:79], v[116:117], %4, v[64:79]\n" \
        EXP4(100, 101, 102, 103, 84, 85, 86, 87) \
        "s_waitcnt lgkmcnt(0)\n" \
        "v_smfmac_f32_32x32x32_bf16 %0, v[122:125], v[104:111], %6\n" \
        EXP4(104, 105, 106, 107, 88, 89, 90, 91) \
        "v_smfmac_f32_32x32x32_bf16 %1, v[118:121], v[96:103], %6\n" \
        PV VRD("ds_read_b128 v[118:121], %9 offset:" #OVL "\n") \
        EXP4(108, 109, 110, 111, 92, 93, 94, 95)
#define LOOPASM(PL, PH, PRE) \
    asm volatile( \
        PRE \
        "ds_read_b128 v[112:115], %7\n" \
        "ds_read_b32 v116, %8\n" \
        "v_mov_b32 v117, 0\n" \
        "v_mov_b32 v104, 0\n v_mov_b32 v105, 0\n v_mov_b32 v106, 0\n v_mov_b32 v107, 0\n" \
        "v_mov_b32 v108, 0\n v_mov_b32 v109, 0\n v_mov_b32 v110, 0\n v_mov_b32 v111, 0\n" \
        "v_mov_b32 v122, 0\n v_mov_b32 v123, 0\n v_mov_b32 v124, 0\n v_mov_b32 v125, 0\n" \
        "v_mov_b32 v118, 0\n v_mov_b32 v119, 0\n v_mov_b32 v120, 0\n v_mov_b32 v121, 0\n" \
        "s_waitcnt lgkmcnt(0)\n" \
        "v_mfma_f32_32x32x16_bf16 v[64:79], v[112:115], %2, 0\n" \
        "v_mfma_f32_32x32x8_bf16 v[64:79], v[116:117], %4, v[64:79]\n" \
        VRD("ds_read_b128 v[118:121], %9\n") \
        "s_nop 7\n s_nop 7\n" \
        "s_mov_b32 s43, m0\n" \
        "1:\n" \
        "s_cmp_lt_u32 %10, 12\n s_cbranch_scc1 3f\n" \
        DMA2 DMA2 \
        "3:\n" \
        "s_movk_i32 s40, 2048\n s_movk_i32 s41, 256\n s_movk_i32 s42, 2432\n" \
        "s_cmp_lg_u32 %10, 9\n s_cbranch_scc1 2f\n" \
        "s_waitcnt vmcnt(0)\n s_barrier\n" \
        "s_mov_b32 s40, 61440\n s_mov_b32 s41, 73984\n s_mov_b32 s42, 58752\n" \
        "2:\n" \
        KEYTILE("", 1024, "", 128, 608, "", 1216, PL, PH) \
        KEYTILE("v_add_u32 %7, s40, %7\n", 0, "v_add_u32 %8, s41, %8\n", 0, 1824, "v_add_u32 %9, s42, %9\n", 0, PL, PH) \
        "s_sub_u32 %10, %10, 1\n s_cmp_lg_u32 %10, 0\n s_cbranch_scc1 1b\n" \
        "s_waitcnt lgkmcnt(0)\n" \
        "v_smfmac_f32_32x32x32_bf16 %1, v[122:125], v[104:111], %6\n" \
        "s_mov_b32 m0, s43\n s_setprio 0\n s_nop 7\n s_nop 7\n" \
        : "+v"(oA), "+v"(oB), "+v"(Q.fA), "+v"(Q.fB), "+v"(Q.rA), "+v"(Q.rB), "+v"(idx), "+v"(ka), "+v"(k2a), "+v"(va), "+s"(cnt), "+s"(pnext) \
        : "s"(vmask), "v"(lane16), "s"(ldsb), "s"(src2) \
        : "v64", "v65", "v66", "v67", "v68", "v69", "v70", "v71", "v72", "v73", "v74", "v75", "v76", "v77", "v78", "v79", \
          "v80", "v81", "v82", "v83", "v84", "v85", "v86", "v87", "v88", "v89", "v90", "v91", "v92", "v93", "v94", "v95", \
          "v96", "v97", "v98", "v99", "v100", "v101", "v102", "v103", "v104", "v105", "v106", "v107", "v108", "v109", "v110", "v111", \
          "v112", "v113", "v114", "v115", "v116", "v117", "v118", "v119", "v120", "v121", "v122", "v123", "v124", "v125", \
          "v126", "s40", "s41", "s42", "s43", "s44", "scc", "memory")
    if (w < 4) { LOOPASM("s_setprio 0\n", "s_setprio 2\n", "s_setprio 2\n"); } else { LOOPASM("", "", "s_setprio 1\n"); }
#undef LOOPASM
#undef KEYTILE
#undef DMA2
#undef VRD
#undef EXP4
}
__device__ __forceinline__ void attn_finish(AttnQ& Q, f32x16& oA, f32x16& oB, rsrc_t rs, int head, int qtA, unsigned lds0, int w, int lane) {
    const int kpart = w >> 2, m = lane & 31, h = lane >> 5;
    float mA = 0.f, mB = 0.f;
    if (!attn_range_ok(oA)) attn_slow(lds0 + kpart * CH_B, lane, Q.fA, Q.rA, oA, mA);
    if (!attn_range_ok(oB)) attn_slow(lds0 + kpart * CH_B, lane, Q.fB, Q.rB, oB, mB);
    LBAR();
    {
        LAS float* xch = (LAS float*)(uintptr_t)(lds0 + w * 3072);
        const f32x16& give = kpart ? oA : oB;
#pragma unroll
        for (int r = 0; r < 11; ++r) xch[r * 64 + lane] = give[r];
        xch[11 * 64 + lane] = kpart ? mA : mB;
    }
    LBAR();
    {
        LAS const float* xin = (LAS const float*)(uintptr_t)(lds0 + (w ^ 4) * 3072);
        const f32x16& keep = kpart ? oB : oA;
        const float m0 = kpart ? mB : mA, m1 = xin[11 * 64 + lane];
        const float mm = fmaxf(m0, m1), f0 = __builtin_amdgcn_exp2f(m0 - mm), f1 = __builtin_amdgcn_exp2f(m1 - mm);
        float v[11];
#pragma unroll
        for (int r = 0; r < 11; ++r) v[r] = keep[r] * f0 + xin[r * 64 + lane] * f1;
        const float inv = 1.0f / v[10];
        const int tb = qtA + kpart;
        const u32x4 o8 = {cvtpk(v[0] * inv, v[1] * inv), cvtpk(v[2] * inv, v[3] * inv), cvtpk(v[4] * inv, v[5] * inv), cvtpk(v[6] * inv, v[7] * inv)};
        st16(rs, (unsigned)WS_OM + ((2 * tb + (m >> 4)) * 16 + (head >> 1)) * 1024 + ((2 * (head & 1) + h) * 16 + (m & 15)) * 16, o8);
        const unsigned rr = cvtpk(v[8] * inv, v[9] * inv);
        const unsigned r0 = __shfl(rr, (lane & 7) * 4), r1 = __shfl(rr, (lane & 7) * 4 + 1), r2 = __shfl(rr, (lane & 7) * 4 + 2), r3 = __shfl(rr, (lane & 7) * 4 + 3);
        if (lane < 8) st16(rs, (unsigned)WS_OR + (tb * 32 + head) * 128 + lane * 16, (u32x4){r0, r1, r2, r3});
    }
}

constexpr int P4_SLAB = 0, P4_ZT = 576 * 144, P4_RED = P4_ZT + 72 * 144;
__device__ __forceinline__ void proj_dma(const u16* __restrict__ wo, int cg, unsigned lds0, int w, int lane) {
#pragma unroll
    for (int i = 0; i < 11; ++i) {
        int p = w + 8 * i; p = p > 80 ? 80 : p;
        const int ck = p * 64 + lane, row = ck / 9, c9 = ck - row * 9;
        glds16(wo + (size_t)row * D + cg * 72 + c9 * 8, lds0 + P4_SLAB + p * 1024);
    }
}
struct GateRegs { u32x4 z0, z1, q0, q1; };
__device__ __forceinline__ void gate_issue(GateRegs& R, rsrc_t rs, const u16* __restrict__ bq, int tb, int cg, int w, int lane) {
    const int tid = threadIdx.x;
    R.z0 = ld16(rs, (unsigned)WS_ZT + (unsigned)(cg * 72 + (tid >> 3)) * (S * 2) + (tb * 64 + (tid & 7) * 8) * 2);
    R.z1 = R.z0;
    if (tid < 64) R.z1 = ld16(rs, (unsigned)WS_ZT + (unsigned)(cg * 72 + 64 + (tid >> 3)) * (S * 2) + (tb * 64 + (tid & 7) * 8) * 2);
    const int G = lane >> 4;
    { const int u = 4 * w + G, tg = u / 9, cb = u - tg * 9; R.q0 = *(const u32x4*)(bq + (size_t)(tb * 64 + tg * 16 + (lane & 15)) * D + cg * 72 + cb * 8); }
    R.q1 = R.q0;
    if (w == 0) { const int u = 32 + G, tg = u / 9, cb = u - tg * 9; R.q1 = *(const u32x4*)(bq + (size_t)(tb * 64 + tg * 16 + (lane & 15)) * D + cg * 72 + cb * 8); }
}
__device__ __forceinline__ void gate_stage(const GateRegs& R, unsigned lds0) {
    const int tid = threadIdx.x;
    ldswr4(lds0 + P4_ZT + (tid >> 3) * 144 + (tid & 7) * 16, R.z0);
    if (tid < 64) ldswr4(lds0 + P4_ZT + (64 + (tid >> 3)) * 144 + (tid & 7) * 16, R.z1);
}
__device__ __forceinline__ void gate_unit(unsigned lds0, u16* __restrict__ out, const u32x4& qq, int u, int tb, int cg, int lane) {
    const int tg = u / 9, cb = u - tg * 9, q = (lane & 15) >> 2, p = lane & 3;
    const unsigned a = lds0 + P4_ZT + (8 * cb + q) * 144 + (16 * tg + 4 * p) * 2;
    const u32x2 zl = __builtin_bit_cast(u32x2, trread(a)), zh = __builtin_bit_cast(u32x2, trread(a + 4 * 144));
    const u32x4 r = {cvtpk(bflo(qq[0]) * bflo(zl[0]), bfhi(qq[0]) * bfhi(zl[0])), cvtpk(bflo(qq[1]) * bflo(zl[1]), bfhi(qq[1]) * bfhi(zl[1])),
                     cvtpk(bflo(qq[2]) * bflo(zh[0]), bfhi(qq[2]) * bfhi(zh[0])), cvtpk(bflo(qq[3]) * bflo(zh[1]), bfhi(qq[3]) * bfhi(zh[1]))};
    *(u32x4*)(out + (size_t)(tb * 64 + tg * 16 + (lane & 15)) * OUTW + D + cg * 72 + cb * 8) = r;
}
__device__ __forceinline__ void gate_finish(const GateRegs& R, unsigned lds0, u16* out, int tb, int cg, int w, int lane) {
    gate_unit(lds0, out, R.q0, 4 * w + (lane >> 4), tb, cg, lane);
    if (w == 0) gate_unit(lds0, out, R.q1, 32 + (lane >> 4), tb, cg, lane);
}
__device__ __forceinline__ void proj_tile(rsrc_t rs, u16* __restrict__ out, int tb64, int cg, unsigned lds0, int w, int lane) {
    const int tt = w & 3, kh = w >> 2, t16 = tb64 * 4 + tt;
    bf16x8 af[9];
#pragma unroll
    for (int s = 0; s < 9; ++s) {
        const int st = kh * 9 + s;
        if (st < 16) af[s] = __builtin_bit_cast(bf16x8, ld16(rs, (unsigned)WS_OM + (t16 * 16 + st) * 1024 + lane * 16));
        else {
            const unsigned ro = (unsigned)WS_OR + ((t16 >> 1) * 32 + 16 * (st - 16) + 4 * (lane >> 4)) * 128 + ((t16 & 1) * 16 + (lane & 15)) * 4;
            af[s] = __builtin_bit_cast(bf16x8, (u32x4){ld4(rs, ro), ld4(rs, ro + 128), ld4(rs, ro + 256), ld4(rs, ro + 384)});
        }
    }
    f32x4 acc[5] = {};
    const int kq = lane >> 4, q = (lane & 15) >> 2, p = lane & 3;
    bf16x8 wf[9][5];
#pragma unroll
    for (int s = 0; s < 9; ++s) {
        const int st = kh * 9 + s;
        int r0, r1;
        if (st < 16) { const int hd = 2 * st + (kq >> 1), hh = kq & 1; r0 = 18 * hd + 4 * hh + q; r1 = r0 + 8; }
        else { const int hd = 16 * (st - 16) + 4 * kq + (q >> 1); r0 = 18 * hd + 16 + (q & 1); r1 = r0 + 36; }
#pragma unroll
        for (int c = 0; c < 5; ++c) {
            const int cc = (c == 4 && p >= 2) ? 64 + (p & 1) * 4 : 16 * c + 4 * p;
            wf[s][c] = cat8(trread(lds0 + P4_SLAB + r0 * 144 + cc * 2), trread(lds0 + P4_SLAB + r1 * 144 + cc * 2));
        }
    }
    SBAR();
#pragma unroll
    for (int s = 0; s < 9; ++s)
#pragma unroll
        for (int c = 0; c < 5; ++c) acc[c] = MFMA16(wf[s][c], af[s], acc[c]);
    LAS f32x4* red = (LAS f32x4*)(uintptr_t)(lds0 + P4_RED + tt * 5120);
    if (kh == 1) {
#pragma unroll
        for (int c = 0; c < 5; ++c) red[c * 64 + lane] = acc[c];
    }
    LBAR();
    if (kh == 0) {
        u16* op = out + (size_t)(t16 * 16 + (lane & 15)) * OUTW + cg * 72 + 4 * kq;
#pragma unroll
        for (int c = 0; c < 5; ++c) {
            const f32x4 v = acc[c] + red[c * 64 + lane];
            if (c < 4 || kq < 2) *(u32x2*)(op + 16 * c) = (u32x2){cvtpk(v[0], v[1]), cvtpk(v[2], v[3])};
        }
    }
}

#define PROLOG extern __shared__ __attribute__((aligned(16))) char lds[]; const unsigned lds0 = (unsigned)(uintptr_t)lds; \
    const int lane = threadIdx.x & 63, w = __builtin_amdgcn_readfirstlane(threadIdx.x >> 6); const rsrc_t rs = ws_rsrc(ws); STAMP_DECL

__global__ __launch_bounds__(512, 2) void k1(const u16* __restrict__ x, const u16* __restrict__ wqkv, const u16* __restrict__ bk, const u16* __restrict__ bv,
                                             const u16* __restrict__ shift_k, char* __restrict__ ws) {
    PROLOG;
    STAMP(0, 0);
    phase1(x, wqkv, bk, bv, shift_k, ws, rs, lds0, w, lane STAMP_PASS);
    STAMP(0, 7);
    STAMP_FLUSH(0);
}
__global__ __launch_bounds__(512, 2) void k2(const u16* __restrict__ ssm_k, const u16* __restrict__ ssm_D, char* __restrict__ ws) {
    PROLOG;
    const int b = blockIdx.x;
    STAMP(1, 0);
    if (b < 224) conv_pair(ssm_k, ssm_D, ws, rs, b, lds0, w, lane STAMP_PASS);
    else { conv_pair(ssm_k, ssm_D, ws, rs, 224 + 2 * (b - 224), lds0, w, lane STAMP_PASS); LBAR(); conv_pair(ssm_k, ssm_D, ws, rs, 225 + 2 * (b - 224), lds0, w, lane STAMP_PASS); }
    STAMP(1, 4);
    STAMP_FLUSH(1);
}
__global__ __launch_bounds__(512, 2) void k3(char* __restrict__ ws) {
    PROLOG;
    const int b = blockIdx.x, head = (b & 7) * 4 + ((b >> 3) & 3), qb = b >> 5, qtA = qb * 8 + (w & 3) * 2;
    STAMP(2, 0);
    AttnQ Q;
    unsigned pf = 0;
    if (K1PF && threadIdx.x < 148) pf = l2touch(ws + WS_KV + (size_t)head * HEAD_B + (size_t)(qb * 148 + threadIdx.x) * 128);
    attn_loadq(Q, ws, head, qtA, lane);
    attn_dma(ws, head, 0, lds0, w, lane);
    ATTN_QWAITBAR(Q, 0);
    attn_fixq(Q, lane);
    STAMP(2, 1);
    f32x16 oA, oB;
    attn_loop(Q, ws, head, lds0, w, lane, oA, oB);
    STAMP(2, 2);
    attn_finish(Q, oA, oB, rs, head, qtA, lds0, w, lane);
    KEEP(pf);
    STAMP(2, 3);
    STAMP_FLUSH(2);
}
__global__ __launch_bounds__(512, 2) void k4(const u16* __restrict__ wo, const u16* __restrict__ bq, char* __restrict__ ws, u16* __restrict__ out) {
    PROLOG;
    const int b = blockIdx.x;
    STAMP(3, 0);
    const int i5 = b & 31, ptb = (b >> 5) * 4 + ((i5 & 7) >> 1), pcg = (i5 & 1) * 4 + (i5 >> 3);
    proj_dma(wo, pcg, lds0, w, lane);
    GateRegs G;
    gate_issue(G, rs, bq, b >> 3, b & 7, w, lane);
    gate_stage(G, lds0);
    WAITBAR(0);
    STAMP(3, 1);
    gate_finish(G, lds0, out, b >> 3, b & 7, w, lane);
    STAMP(3, 2);
    proj_tile(rs, out, ptb, pcg, lds0, w, lane);
    STAMP(3, 3);
    STAMP_FLUSH(3);
}

extern "C" void kernel_launch(void* const* d_in, const int* in_sizes, int n_in, void* d_out, int out_size, void* d_ws, size_t ws_size,
                              hipStream_t stream) {
    const u16* x = (const u16*)d_in[0];
    const u16* wqkv = (const u16*)d_in[1];
    const u16* wo = (const u16*)d_in[2];
    const u16* bq = (const u16*)d_in[3];
    const u16* bk = (const u16*)d_in[4];
    const u16* bv = (const u16*)d_in[5];
    const u16* shift_k = (const u16*)d_in[6];
    const u16* ssm_k = (const u16*)d_in[7];
    const u16* ssm_D = (const u16*)d_in[8];
    char* ws = (char*)d_ws;
    (void)hipFuncSetAttribute((const void*)k1, hipFuncAttributeMaxDynamicSharedMemorySize, LDS_BYTES);
    (void)hipFuncSetAttribute((const void*)k2, hipFuncAttributeMaxDynamicSharedMemorySize, LDS_BYTES);
    (void)hipFuncSetAttribute((const void*)k3, hipFuncAttributeMaxDynamicSharedMemorySize, LDS_BYTES);
    (void)hipFuncSetAttribute((const void*)k4, hipFuncAttributeMaxDynamicSharedMemorySize, LDS_BYTES);
    k1<<<256, 512, LDS_BYTES, stream>>>(x, wqkv, bk, bv, shift_k, ws);
    k2<<<256, 512, LDS_BYTES, stream>>>(ssm_k, ssm_D, ws);
    k3<<<256, 512, LDS_BYTES, stream>>>(ws);
    k4<<<256, 512, LDS_BYTES, stream>>>(wo, bq, ws, (u16*)d_out);
}
```

```cpp
#include <hip/hip_runtime.h>
#include <hip/hip_bf16.h>
#include <stdint.h>

#define LAS __attribute__((address_space(3)))
typedef __attribute__((ext_vector_type(8))) __bf16 bf16x8;
typedef __attribute__((ext_vector_type(16))) __bf16 bf16x16;
typedef __attribute__((ext_vector_type(4))) short s16x4;
typedef __attribute__((ext_vector_type(8))) short s16x8;
typedef __attribute__((ext_vector_type(16))) float f32x16;
typedef __attribute__((ext_vector_type(8))) float f32x8;
typedef __attribute__((ext_vector_type(4))) float f32x4;
typedef __attribute__((ext_vector_type(4))) unsigned u32x4;
typedef __attribute__((ext_vector_type(2))) unsigned u32x2;
typedef unsigned short u16;

constexpr int S = 2048, D = 576, NH = 32;
constexpr int OUTW = 2 * D;
constexpr int CH_K2 = 16384, CH_VT = 18432, CH_B = 37888, HEAD_B = 4 * CH_B;
constexpr int QT_B = 1152, QH_B = 64 * QT_B;
constexpr size_t WS_FLAG = 0;
constexpr unsigned FL_GEN = 0, FL_KV = 1024, FL_QKV = 2560, FL_Z = 3584, FL_O = 4608;
constexpr size_t WS_KV = 8192;
constexpr size_t WS_Q = WS_KV + (size_t)NH * HEAD_B;
constexpr size_t WS_OM = WS_Q + (size_t)NH * QH_B;
constexpr size_t WS_OR = WS_OM + (size_t)128 * 16 * 1024;
constexpr size_t WS_KVT = WS_OR + (size_t)64 * 32 * 128;
constexpr size_t WS_ZT = WS_KVT + (size_t)S * D * 2;
constexpr size_t WS_END = WS_ZT + (size_t)S * D * 2;
constexpr float QSCALE = 0.23570226039551584f * 1.4426950408889634f;
constexpr int LDS_BYTES = 158720;

#define STAMP_DECL
#define STAMP(kid, pt)
#define STAMP_FLUSH(kid)
#define STAMP_ARG
#define STAMP_PASS
#define SBAR() __builtin_amdgcn_sched_barrier(0)
#define MFMA32(a, b, c) __builtin_amdgcn_mfma_f32_32x32x16_bf16(a, b, c, 0, 0, 0)
#define MFMA16(a, b, c) __builtin_amdgcn_mfma_f32_16x16x32_bf16(a, b, c, 0, 0, 0)
#define WAITBAR(V) asm volatile("s_waitcnt vmcnt(" #V ") lgkmcnt(0)\n\ts_barrier" ::: "memory")
#define LBAR() asm volatile("s_waitcnt lgkmcnt(0)\n\ts_barrier" ::: "memory")

__device__ __forceinline__ void glds16(const void* g, unsigned lds_base) {
    unsigned sv;
    asm volatile("s_mov_b32 %0, m0\n\ts_mov_b32 m0, %2\n\ts_nop 0\n\tglobal_load_lds_dwordx4 %1, off\n\ts_mov_b32 m0, %0"
                 : "=&s"(sv) : "v"(g), "s"(lds_base) : "memory");
}
__device__ __forceinline__ unsigned cvtpk(float lo, float hi) {
    typedef __attribute__((ext_vector_type(2))) float f2; typedef __attribute__((ext_vector_type(2))) __bf16 b2;
    f2 v = {lo, hi};
    return __builtin_bit_cast(unsigned, __builtin_convertvector(v, b2));
}
__device__ __forceinline__ float bflo(unsigned u) { return __uint_as_float(u << 16); }
__device__ __forceinline__ float bfhi(unsigned u) { return __uint_as_float(u & 0xffff0000u); }
__device__ __forceinline__ float rbf(float x) { return bflo(cvtpk(x, 0.f)); }
__device__ __forceinline__ s16x4 trread(unsigned a) {
    return __builtin_bit_cast(s16x4, __builtin_amdgcn_ds_read_tr16_b64_v4i16((LAS s16x4*)(uintptr_t)a));
}
__device__ __forceinline__ bf16x8 cat8(s16x4 a, s16x4 b) { return __builtin_bit_cast(bf16x8, (s16x8)__builtin_shufflevector(a, b, 0, 1, 2, 3, 4, 5, 6, 7)); }
__device__ __forceinline__ bf16x8 ldsrd128(unsigned a) { return *(LAS const bf16x8*)(uintptr_t)a; }
__device__ __forceinline__ u32x4 ldsrdu4(unsigned a) { return *(LAS const u32x4*)(uintptr_t)a; }
__device__ __forceinline__ u32x2 ldsrdu2(unsigned a) { return *(LAS const u32x2*)(uintptr_t)a; }
__device__ __forceinline__ void ldswr4(unsigned a, u32x4 v) { *(LAS u32x4*)(uintptr_t)a = v; }
__device__ __forceinline__ void ldswr2(unsigned a, u32x2 v) { *(LAS u32x2*)(uintptr_t)a = v; }
__device__ __forceinline__ void ldswr1(unsigned a, unsigned v) { *(LAS unsigned*)(uintptr_t)a = v; }

__device__ __forceinline__ unsigned l2touch(const void* p) { unsigned r; asm volatile("global_load_dword %0, %1, off" : "=v"(r) : "v"(p) : "memory"); return r; }
#define KEEP(x) asm volatile("" :: "v"(x))

typedef __amdgpu_buffer_rsrc_t rsrc_t;
__device__ __forceinline__ rsrc_t ws_rsrc(char* ws) { return __builtin_amdgcn_make_buffer_rsrc(ws, 0, (int)WS_END, 0x00020000); }
__device__ __forceinline__ void st16(rsrc_t r, unsigned off, u32x4 v) { __builtin_amdgcn_raw_buffer_store_b128(v, r, off, 0, 16); }
__device__ __forceinline__ void st8(rsrc_t r, unsigned off, u32x2 v) { __builtin_amdgcn_raw_buffer_store_b64(v, r, off, 0, 16); }
__device__ __forceinline__ void st4(rsrc_t r, unsigned off, unsigned v) { __builtin_amdgcn_raw_buffer_store_b32(v, r, off, 0, 16); }
__device__ __forceinline__ u32x4 ld16(rsrc_t r, unsigned off) { return __builtin_amdgcn_raw_buffer_load_b128(r, off, 0, 16); }
__device__ __forceinline__ unsigned ld4(rsrc_t r, unsigned off) { return __builtin_amdgcn_raw_buffer_load_b32(r, off, 0, 16); }

__device__ __forceinline__ void publish(rsrc_t rs, unsigned off, unsigned epoch) { if (threadIdx.x == 0) st4(rs, off, epoch); }
__device__ __forceinline__ void poll_flags(rsrc_t rs, unsigned off, int nwords, unsigned epoch, int lane) {
    for (int spin = 0; spin < (1 << 18); ++spin) {
        bool ok = true;
        if (lane * 4 < nwords) { const u32x4 v = ld16(rs, off + lane * 16); ok = v[0] == epoch && v[1] == epoch && v[2] == epoch && v[3] == epoch; }
        if (__all(ok)) break;
        __builtin_amdgcn_s_sleep(4);
        asm volatile("" ::: "memory");
    }
    asm volatile("" ::: "memory");
}

constexpr int KVS_K = 0, KVS_W = 67 * 144, KVS_T = KVS_W + 512, KVS_B = KVS_T + 64 * 144;
struct KvRegs { u32x4 k, v, x; };
__device__ __forceinline__ u32x4 gload16(const void* p) {
    u32x4 r; asm volatile("global_load_dwordx4 %0, %1, off" : "=v"(r) : "v"(p) : "memory"); return r;
}
__device__ __forceinline__ void kv_issue(KvRegs& R, const u16* __restrict__ bk, const u16* __restrict__ bv, const u16* __restrict__ shift_k, int tb, int cg) {
    const int tid = threadIdx.x, t = tb * 64 + (tid >> 3), c = cg * 64 + (tid & 7) * 8;
    R.k = gload16(bk + (size_t)t * D + c);
    R.v = gload16(bv + (size_t)t * D + c);
    const u16* xp = bk + c;
    if (tid < 24) { const int th = tb * 64 - 3 + (tid >> 3); xp = bk + (size_t)(th < 0 ? 0 : th) * D + c; }
    else if (tid >= 64 && tid < 96) xp = shift_k + cg * 256 + (tid - 64) * 8;
    R.x = gload16(xp);
}
#define KV_WAIT(R, N) asm volatile("s_waitcnt vmcnt(" #N ")" : "+v"(R.k), "+v"(R.v), "+v"(R.x) :: "memory")
__device__ __forceinline__ void kv_stage(const KvRegs& R, int tb, unsigned sc) {
    const int tid = threadIdx.x;
    ldswr4(sc + KVS_K + ((tid >> 3) + 3) * 144 + (tid & 7) * 16, R.k);
    if (tid < 24) ldswr4(sc + KVS_K + (tid >> 3) * 144 + (tid & 7) * 16, (tb * 64 - 3 + (tid >> 3) < 0) ? (u32x4){0u, 0u, 0u, 0u} : R.x);
    else if (tid >= 64 && tid < 96) ldswr4(sc + KVS_W + (tid - 64) * 16, R.x);
}
__device__ __forceinline__ float dot2bf(unsigned a, unsigned b, float c) {
    typedef __attribute__((ext_vector_type(2))) __bf16 b2;
    return __builtin_amdgcn_fdot2_f32_bf16(__builtin_bit_cast(b2, a), __builtin_bit_cast(b2, b), c, false);
}
__device__ __forceinline__ unsigned kv_pair(unsigned k0, unsigned k1, unsigned k2, unsigned k3, unsigned vv, unsigned wa0, unsigned wa1, unsigned wb0, unsigned wb1) {
    const float sl = dot2bf(__builtin_amdgcn_perm(k2, k3, 0x05040100u), wa0, dot2bf(__builtin_amdgcn_perm(k0, k1, 0x05040100u), wa1, 0.f));
    const float sh = dot2bf(__builtin_amdgcn_perm(k2, k3, 0x07060302u), wb0, dot2bf(__builtin_amdgcn_perm(k0, k1, 0x07060302u), wb1, 0.f));
    const unsigned s = cvtpk(sl, sh);
    return cvtpk(bflo(s) * bflo(vv), bfhi(s) * bfhi(vv));
}
__device__ __forceinline__ void kv_compute(const KvRegs& R, unsigned sc) {
    const int tid = threadIdx.x, trow = tid >> 3, ch = tid & 7;
    const unsigned ka = sc + KVS_K + trow * 144 + ch * 16;
    const u32x4 k0 = ldsrdu4(ka), k1 = ldsrdu4(ka + 144), k2 = ldsrdu4(ka + 288);
    const unsigned wa = sc + KVS_W + ch * 64;
    const u32x4 w0 = ldsrdu4(wa), w1 = ldsrdu4(wa + 16), w2 = ldsrdu4(wa + 32), w3 = ldsrdu4(wa + 48);
    const u32x4 r = {kv_pair(k0[0], k1[0], k2[0], R.k[0], R.v[0], w0[0], w0[1], w0[2], w0[3]),
                     kv_pair(k0[1], k1[1], k2[1], R.k[1], R.v[1], w1[0], w1[1], w1[2], w1[3]),
                     kv_pair(k0[2], k1[2], k2[2], R.k[2], R.v[2], w2[0], w2[1], w2[2], w2[3]),
                     kv_pair(k0[3], k1[3], k2[3], R.k[3], R.v[3], w3[0], w3[1], w3[2], w3[3])};
    const int row = (trow & ~7) | (7 - (trow & 7));
    ldswr4(sc + KVS_T + row * 144 + ch * 16, r);
}
__device__ __forceinline__ void kv_store(rsrc_t rs, int tb, int cg, unsigned sc, int w, int lane) {
    const int G = lane >> 4, q = (lane & 15) >> 2, p = lane & 3;
    const int cw = (w >> 1) * 16;
    s16x4 a[4];
#pragma unroll
    for (int u = 0; u < 4; ++u) a[u] = trread(sc + KVS_T + (16 * G + 4 * u + q) * 144 + (cw + 4 * p) * 2);
    const int i = lane & 15;
    if ((i >> 3) == (w & 1)) {
        const unsigned off = (unsigned)WS_KVT + (unsigned)(cg * 64 + cw + i) * (S * 2) + (tb * 64 + 16 * G) * 2;
        st16(rs, off, __builtin_bit_cast(u32x4, (s16x8)__builtin_shufflevector(a[0], a[1], 0, 1, 2, 3, 4, 5, 6, 7)));
        st16(rs, off + 16, __builtin_bit_cast(u32x4, (s16x8)__builtin_shufflevector(a[2], a[3], 0, 1, 2, 3, 4, 5, 6, 7)));
    }
}

#define ATEXP 0
#define NOKV 0
#define K1PF 0
constexpr int G1_STAGE = 32768, G1_SC = 4 * G1_STAGE;

template <bool ISV>
__device__ __forceinline__ void qkv_tile(const u16* __restrict__ x, const u16* __restrict__ wqkv, char* __restrict__ ws, rsrc_t rs,
                                         int mb, int ng, unsigned lds0, int w, int lane,
                                         const u16* __restrict__ bk, const u16* __restrict__ bv, const u16* __restrict__ shift_k, int kvtb, int kvcg, unsigned epoch STAMP_ARG) {
    const int ctl = w & 3, tb0 = (w >> 2) * 2;
    const bool active = (ng * 4 + ctl) < 54;
    const u16* xsrc[2]; const u16* wsrc[2];
#pragma unroll
    for (int i = 0; i < 2; ++i) {
        const int p = w * 2 + i;
        const int xr = 8 * p + (lane >> 3), xc = (lane & 7) ^ ((xr >> 1) & 7);
        xsrc[i] = x + (size_t)(mb * 128 + xr) * D + xc * 8;
        const int wr = 4 * p + (lane >> 4), wc = (lane & 15) ^ (((wr & 3) << 2) | ((wr >> 2) & 3));
        int col = ng * 128 + wc * 8; col = col > 1720 ? 1720 : col;
        wsrc[i] = wqkv + (size_t)wr * (3 * D) + col;
    }
    auto issue1 = [&](int c, int pc) {
        const unsigned st = lds0 + (c & 3) * G1_STAGE;
        const int i = pc >> 1;
        if (pc & 1) glds16(wsrc[i] + (size_t)c * 64 * (3 * D), st + 16384 + (w * 2 + i) * 1024);
        else glds16(xsrc[i] + c * 64, st + (w * 2 + i) * 1024);
    };
    auto issue = [&](int c) { issue1(c, 0); issue1(c, 1); issue1(c, 2); issue1(c, 3); };
    const int r = lane & 31, h = lane >> 5;
    unsigned xa[4];
#pragma unroll
    for (int s = 0; s < 4; ++s) xa[s] = lds0 + (tb0 * 32 + r) * 128 + (((2 * s + h) ^ ((r >> 1) & 7)) << 4);
    unsigned wa[2];
    {   const int blk = (lane >> 4) & 1, q = (lane & 15) >> 2, p = lane & 3;
#pragma unroll
        for (int u = 0; u < 2; ++u) {
            const int row = 8 * h + 4 * u + q, ch = 4 * ctl + 2 * blk + (p >> 1);
            wa[u] = lds0 + 16384 + row * 256 + ((ch ^ (((row & 3) << 2) | ((row >> 2) & 3))) << 4) + 8 * (p & 1);
        } }
    f32x16 acc0 = {}, acc1 = {};
    KvRegs R;
    unsigned pf = 0;
    if (K1PF) {
        const int b = blockIdx.x, xcd = b & 7, li = (b >> 3) * 396 + (int)threadIdx.x;
        if (threadIdx.x < 396) {
            const u16* p;
            if (li < 4608) p = x + (size_t)((xcd >> 1) * 512 + li / 9) * D + (li % 9) * 64;
            else { const int l2 = li - 4608; int col = (xcd & 1) * 896 + (l2 % 14) * 64; col = col > 1664 ? 1664 : col; p = wqkv + (size_t)(l2 / 14) * (3 * D) + col; }
            pf = l2touch(p);
        }
    }
    issue(0);
    kv_issue(R, bk, bv, shift_k, kvtb, kvcg);
    issue(1);
    struct Frags { bf16x8 w[4], x0[4], x1[4]; };
    Frags fa, fb;
    auto rd = [&](Frags& f, int c, int s) {
        const unsigned st = (c & 3) * G1_STAGE;
        f.w[s] = cat8(trread(wa[0] + st + s * 4096), trread(wa[1] + st + s * 4096));
        f.x0[s] = ldsrd128(xa[s] + st); f.x1[s] = ldsrd128(xa[s] + st + 4096);
    };
    auto mm = [&](const Frags& f, int s) {
        if (ISV) { acc0 = MFMA32(f.x0[s], f.w[s], acc0); acc1 = MFMA32(f.x1[s], f.w[s], acc1); }
        else     { acc0 = MFMA32(f.w[s], f.x0[s], acc0); acc1 = MFMA32(f.w[s], f.x1[s], acc1); }
    };
#define G1_STEP(CUR, NXT, c, N, I0, I1) do { WAITBAR(N); SBAR(); if ((I0) < 9) issue(I0); if ((I1) < 9) issue(I1); SBAR(); \
        if (active) { _Pragma("unroll") for (int s = 0; s < 4; ++s) { rd(NXT, (c) + 1, s); SBAR(); mm(CUR, s); SBAR(); } } } while (0)
    const unsigned sc = lds0 + G1_SC;
    KV_WAIT(R, 4);
    kv_stage(R, kvtb, sc);
    WAITBAR(4);
    if (active) { _Pragma("unroll") for (int s = 0; s < 4; ++s) rd(fa, 0, s); }
    issue(2);
    kv_compute(R, sc);
    G1_STEP(fa, fb, 0, 4, 3, 4);
    kv_store(rs, kvtb, kvcg, sc, w, lane);
    G1_STEP(fb, fa, 1, 10, 5, 9);
    G1_STEP(fa, fb, 2, 10, 6, 9);
    G1_STEP(fb, fa, 3, 8, 7, 9);
    publish(rs, FL_KV + (kvcg * 32 + kvtb) * 4, epoch);
    G1_STEP(fa, fb, 4, 8, 8, 9);
    G1_STEP(fb, fa, 5, 8, 9, 9);
    G1_STEP(fa, fb, 6, 4, 9, 9);
    G1_STEP(fb, fa, 7, 0, 9, 9);
    if (active) { _Pragma("unroll") for (int s = 0; s < 4; ++s) mm(fa, s); }
    KEEP(pf);
    STAMP(0, 1);
    const int ct = ng * 4 + ctl;
    if (ISV) {
        if (active) {
#pragma unroll
            for (int i = 0; i < 2; ++i) {
                const f32x16& acc = i ? acc1 : acc0;
                const int T = mb * 4 + tb0 + i;
                const int col = (ct - 36) * 32 + r, head = col / 18, d = col - head * 18;
                const unsigned vp = (unsigned)WS_KV + head * HEAD_B + (((T >> 4) & 1) * 2 + (T >> 5)) * CH_B + CH_VT + (2 * (T & 15)) * 608 + h * 304 + d * 16;
#pragma unroll
                for (int g = 0; g < 2; ++g) {
                    const u32x4 o = {cvtpk(acc[8 * g], acc[8 * g + 1]), cvtpk(acc[8 * g + 2], acc[8 * g + 3]),
                                     cvtpk(acc[8 * g + 4], acc[8 * g + 5]), cvtpk(acc[8 * g + 6], acc[8 * g + 7])};
                    st16(rs, vp + g * 608, o);
                    if (d == 17) st16(rs, vp + g * 608 + 16, (u32x4){0x3f803f80u, 0x3f803f80u, 0x3f803f80u, 0x3f803f80u});
                }
            }
        }
    } else {
        LBAR();
        if (active) {
            const bool isq = ct < 18;
            const float scl = isq ? QSCALE : 1.0f;
            const int row = isq ? r : ((r & 16) | ((r & 4) << 1) | ((r & 8) >> 1) | (r & 3));
#pragma unroll
            for (int i = 0; i < 2; ++i) {
                const f32x16& acc = i ? acc1 : acc0;
#pragma unroll
                for (int g = 0; g < 4; ++g)
                    ldswr2(lds0 + (tb0 + i) * 8192 + (8 * ctl + 2 * g + h) * 256 + row * 8,
                           (u32x2){cvtpk(acc[4 * g] * scl, acc[4 * g + 1] * scl), cvtpk(acc[4 * g + 2] * scl, acc[4 * g + 3] * scl)});
            }
        }
        LBAR();
        const int g0 = ng * 32;
#pragma unroll
        for (int k5 = 0; k5 < 5; ++k5) {
            const int it = w + 8 * k5, tt = it / 10, cand = (it % 10) * 2 + h;
            const int blk0 = g0 / 9, blk = blk0 + (cand >> 2), cpos = cand & 3;
            const int pos = (cpos & 1) * 2 + (cpos >> 1) * 5;
            const int gq = blk * 9 + pos, lq = gq - g0;
            const bool in0 = lq >= 0 && lq < 32 && gq < 288, in1 = lq + 1 >= 0 && lq + 1 < 32 && gq + 1 < 288;
            if (in0 || in1) {
                const bool isq = gq < 144;
                const int pb = isq ? blk : blk - 16, head = 2 * pb + (cpos >> 1), hp = cpos & 1;
                const int T = mb * 4 + tt;
                const unsigned dst = isq ? (unsigned)WS_Q + head * QH_B + T * QT_B + hp * 512 + r * 16
                                         : (unsigned)WS_KV + head * HEAD_B + (((T >> 4) & 1) * 2 + (T >> 5)) * CH_B + (T & 15) * 1024 + hp * 512 + r * 16;
                const unsigned sa = lds0 + tt * 8192 + lq * 256 + r * 8;
                if (in0 && in1) { const u32x2 a = ldsrdu2(sa), b = ldsrdu2(sa + 256); st16(rs, dst, (u32x4){a[0], a[1], b[0], b[1]}); }
                else if (in0) st8(rs, dst, ldsrdu2(sa));
                else st8(rs, dst + 8, ldsrdu2(sa + 256));
            }
        }
        {
            const int blk = g0 / 9 + w, gq = blk * 9 + 4, lq = gq - g0;
            if (w < 5 && lq >= 0 && lq < 32 && gq < 288) {
                const int tt = lane >> 4, par = (lane >> 3) & 1, rq = lane & 7;
                const bool isq = gq < 144;
                const int head = 2 * (isq ? blk : blk - 16) + par, T = mb * 4 + tt;
                const unsigned sa = lds0 + tt * 8192 + lq * 256 + rq * 32 + par * 4;
                const u32x4 v = {*(LAS const unsigned*)(uintptr_t)sa, *(LAS const unsigned*)(uintptr_t)(sa + 8),
                                 *(LAS const unsigned*)(uintptr_t)(sa + 16), *(LAS const unsigned*)(uintptr_t)(sa + 24)};
                const unsigned dst = isq ? (unsigned)WS_Q + head * QH_B + T * QT_B + 1024 + rq * 16
                                         : (unsigned)WS_KV + head * HEAD_B + (((T >> 4) & 1) * 2 + (T >> 5)) * CH_B + CH_K2 + (T & 15) * 128 + rq * 16;
                st16(rs, dst, v);
            }
        }
    }
}

__device__ __forceinline__ void phase1(const u16* x, const u16* wqkv, const u16* bk, const u16* bv, const u16* shift_k, char* ws, rsrc_t rs,
                                       unsigned lds0, int w, int lane, unsigned epoch STAMP_ARG) {
    const int b = blockIdx.x;
    if (b >= 224) {
        const int t = 224 + (b - 224) * 2;
        unsigned pf = 0;
        if (K1PF) {
            const int xcd = b & 7, li = (b >> 3) * 396 + (int)threadIdx.x;
            if (threadIdx.x < 396) {
                const int l2 = li - 4608; int col = (xcd & 1) * 896 + (l2 % 14) * 64; col = col > 1664 ? 1664 : col;
                pf = l2touch(wqkv + (size_t)(l2 / 14) * (3 * D) + col);
            }
        }
        KvRegs R0, R1;
        kv_issue(R0, bk, bv, shift_k, t & 31, t >> 5); kv_issue(R1, bk, bv, shift_k, (t + 1) & 31, (t + 1) >> 5);
        KV_WAIT(R0, 3); kv_stage(R0, t & 31, lds0);
        KV_WAIT(R1, 0); kv_stage(R1, (t + 1) & 31, lds0 + KVS_B);
        LBAR();
        kv_compute(R0, lds0); kv_compute(R1, lds0 + KVS_B);
        LBAR();
        kv_store(rs, t & 31, t >> 5, lds0, w, lane); kv_store(rs, (t + 1) & 31, (t + 1) >> 5, lds0 + KVS_B, w, lane);
        KEEP(pf);
        WAITBAR(0);
        publish(rs, FL_KV + t * 4, epoch);
        if (threadIdx.x == 0) st4(rs, FL_KV + (t + 1) * 4, epoch);
        return;
    }
    const int xcd = b & 7, i = b >> 3, mb = (xcd >> 1) * 4 + (i & 3), ng = (xcd & 1) * 7 + (i >> 2);
    if (ng >= 9) qkv_tile<true>(x, wqkv, ws, rs, mb, ng, lds0, w, lane, bk, bv, shift_k, b & 31, b >> 5, epoch STAMP_PASS);
    else qkv_tile<false>(x, wqkv, ws, rs, mb, ng, lds0, w, lane, bk, bv, shift_k, b & 31, b >> 5, epoch STAMP_PASS);
}

constexpr int CV_U = 33280, CV_B = CV_U + 7600;
constexpr int CV_BASE = LDS_BYTES - 2 * CV_B;

struct ConvRegs { u32x4 h0, h1; unsigned h2; u32x4 u; unsigned e[8]; };
__device__ __forceinline__ void conv_issue_h(ConvRegs& R, const u16* __restrict__ ssm_k, int c) {
    const int i = threadIdx.x & 255;
    const rsrc_t hr = __builtin_amdgcn_make_buffer_rsrc((void*)(ssm_k + (size_t)c * S), 0, S * 2, 0x00020000);
    const unsigned off = i < 4 ? 0u : (unsigned)(((9 * i - 32) >> 1) * 4);
    if (i < 64) {
#pragma unroll
        for (int j = 0; j < 8; ++j) R.e[j] = __builtin_amdgcn_raw_buffer_load_b16(hr, (unsigned)((i - 32 + j) * 2), 0, 0);
    }
    R.h0 = __builtin_amdgcn_raw_buffer_load_b128(hr, off, 0, 0);
    R.h1 = __builtin_amdgcn_raw_buffer_load_b128(hr, off + 16, 0, 0);
    R.h2 = __builtin_amdgcn_raw_buffer_load_b32(hr, off + 32, 0, 0);
}
__device__ __forceinline__ void conv_issue_u(ConvRegs& R, rsrc_t rs, int c) { R.u = ld16(rs, (unsigned)WS_KVT + (unsigned)c * (S * 2) + (threadIdx.x & 255) * 16); }
__device__ __forceinline__ void conv_build(const ConvRegs& R, unsigned base) {
    const int i = threadIdx.x & 255;
    if (i < 36) ldswr4(base + i * 16, (u32x4){R.e[0] | (R.e[1] << 16), R.e[2] | (R.e[3] << 16), R.e[4] | (R.e[5] << 16), R.e[6] | (R.e[7] << 16)});
    if (i >= 4 && i < 232) {
        const unsigned sh = (i & 1) * 16;
        const unsigned dw[9] = {R.h0[0], R.h0[1], R.h0[2], R.h0[3], R.h1[0], R.h1[1], R.h1[2], R.h1[3], R.h2};
        unsigned A[8], Bq[7];
#pragma unroll
        for (int n = 0; n < 8; ++n) A[n] = __builtin_amdgcn_alignbit(dw[n + 1], dw[n], sh);
#pragma unroll
        for (int n = 0; n < 7; ++n) Bq[n] = __builtin_amdgcn_alignbit(A[n + 1], A[n], 16);
        const unsigned a = base + 9 * i * 16;
#pragma unroll
        for (int u = 0; u < 9; ++u) {
            if (i < 231 || u < 1) {
                if (u & 1) ldswr4(a + u * 16, (u32x4){Bq[u >> 1], Bq[(u >> 1) + 1], Bq[(u >> 1) + 2], Bq[(u >> 1) + 3]});
                else ldswr4(a + u * 16, (u32x4){A[u >> 1], A[(u >> 1) + 1], A[(u >> 1) + 2], A[(u >> 1) + 3]});
            }
        }
    }
    if (i < 155) ldswr4(base + CV_U + i * 16, (u32x4){0u, 0u, 0u, 0u});
}
__device__ __forceinline__ void conv_build_u(const ConvRegs& R, unsigned base) {
    const int i = threadIdx.x & 255;
    ldswr4(base + CV_U + (31 + (i >> 2)) * 80 + (i & 3) * 16, R.u);
}
template <int JB, int JE>
__device__ __forceinline__ void conv_range(unsigned aaddr, unsigned baddr, f32x16& d0, f32x16& d1) {
    auto boff = [](int j) { const int c2 = 1040 - 16 * j; return ((c2 >> 5) + 31) * 80 + (c2 & 16) * 2; };
    constexpr int PD = 4;
    bf16x8 a[PD], b1[PD], b0[PD];
#pragma unroll
    for (int j = JB; j < JB + PD; ++j) { a[(j - JB) % PD] = ldsrd128(aaddr + j * 256); b1[(j - JB) % PD] = ldsrd128(baddr + boff(j)); if (j < 64) b0[(j - JB) % PD] = ldsrd128(baddr + boff(j) - 2560); }
    SBAR();
#pragma unroll
    for (int j = JB; j < JE; ++j) {
        const int k = (j - JB) % PD;
        d1 = MFMA32(a[k], b1[k], d1);
        if (j < 64) d0 = MFMA32(a[k], b0[k], d0);
        SBAR();
        if (j + PD < JE) { a[k] = ldsrd128(aaddr + (j + PD) * 256); b1[k] = ldsrd128(baddr + boff(j + PD)); if (j + PD < 64) b0[k] = ldsrd128(baddr + boff(j + PD) - 2560); }
        SBAR();
    }
}
__device__ __forceinline__ void conv_main(unsigned base, int w, int lane, f32x16& d0, f32x16& d1, bool poll, rsrc_t rs, unsigned epoch) {
    const int n = lane & 31, hh = lane >> 5, wg = w & 3;
    const unsigned aaddr = base + (n - 8 * hh + 9) * 16, baddr = base + CV_U + n * 80 + hh * 16;
    d0 = f32x16{}; d1 = f32x16{};
    if (wg == 0) conv_range<0, 26>(aaddr, baddr, d0, d1);
    else if (wg == 1) conv_range<26, 52>(aaddr, baddr, d0, d1);
    else if (wg == 2) conv_range<52, 88>(aaddr, baddr, d0, d1);
    else {
        conv_range<88, 108>(aaddr, baddr, d0, d1);
        if (poll && w == 3) { poll_flags(rs, FL_QKV, 224, epoch, lane); asm volatile("buffer_inv sc1" ::: "memory"); }
        conv_range<108, 128>(aaddr, baddr, d0, d1);
    }
}
__device__ __forceinline__ void conv_partials(unsigned base, int wg, int lane, const f32x16& d0, const f32x16& d1) {
    const int n = lane & 31, hh = lane >> 5;
    const unsigned p = base + wg * 9216 + (36 * n + 4 * hh) * 4;
#pragma unroll
    for (int g = 0; g < 4; ++g) {
        if (wg < 3) *(LAS f32x4*)(uintptr_t)(p + g * 32) = (f32x4){d0[4 * g], d0[4 * g + 1], d0[4 * g + 2], d0[4 * g + 3]};
        *(LAS f32x4*)(uintptr_t)(p + 4608 + g * 32) = (f32x4){d1[4 * g], d1[4 * g + 1], d1[4 * g + 2], d1[4 * g + 3]};
    }
}
__device__ __forceinline__ void conv_finish(unsigned base, rsrc_t rs, const u16* __restrict__ ssm_D, int c, const u32x4& uu) {
    const int i = threadIdx.x & 255;
    const unsigned p = base + (8 * i + 4 * (i >> 2)) * 4;
    f32x4 lo = *(LAS const f32x4*)(uintptr_t)p, hi = *(LAS const f32x4*)(uintptr_t)(p + 16);
#pragma unroll
    for (int k = 1; k < 4; ++k)
        if (k < 3 || i >= 128) { lo += *(LAS const f32x4*)(uintptr_t)(p + k * 9216); hi += *(LAS const f32x4*)(uintptr_t)(p + k * 9216 + 16); }
    const float dd = bflo(ssm_D[c]);
    const float y[8] = {lo[0], lo[1], lo[2], lo[3], hi[0], hi[1], hi[2], hi[3]};
    unsigned o[4];
#pragma unroll
    for (int j = 0; j < 4; ++j) {
        const float u0 = bfhi(uu[3 - j]), u1 = bflo(uu[3 - j]);
        o[j] = cvtpk(rbf(y[2 * j]) + rbf(u0 * dd), rbf(y[2 * j + 1]) + rbf(u1 * dd));
    }
    st16(rs, (unsigned)WS_ZT + (unsigned)c * (S * 2) + i * 16, (u32x4){o[0], o[1], o[2], o[3]});
}
__device__ __forceinline__ unsigned attn_tile_off(int t) { return t < 16 ? 0u : 2u * CH_B; }
__device__ __forceinline__ void attn_slow(unsigned base, int lane, bf16x8 qf, s16x4 q2, f32x16& o, float& mrow) {
    const int m_ = lane & 31, h = lane >> 5, vrow = (m_ < 19) ? m_ : ((m_ == 22) ? 18 : 0);
    float m = -3.0e38f;
    o = f32x16{};
    for (int t = 0; t < 32; ++t) {
        const unsigned cb = base + attn_tile_off(t), tl = t & 15;
        const bf16x8 kf = ldsrd128(cb + tl * 1024 + lane * 16);
        const unsigned k2 = *(LAS const unsigned*)(uintptr_t)(cb + CH_K2 + tl * 128 + m_ * 4);
        f32x16 s = {};
        s = MFMA32(kf, qf, s);
        s = __builtin_amdgcn_mfma_f32_32x32x8bf16_1k(__builtin_bit_cast(s16x4, (u32x2){k2, 0u}), q2, s, 0, 0, 0);
        float tm = s[0];
#pragma unroll
        for (int r = 1; r < 16; ++r) tm = fmaxf(tm, s[r]);
        tm = fmaxf(tm, __shfl_xor(tm, 32));
        const float mn = fmaxf(m, tm);
        const float f = __builtin_amdgcn_exp2f(m - mn);
        m = mn;
#pragma unroll
        for (int r = 0; r < 16; ++r) { o[r] *= f; s[r] = __builtin_amdgcn_exp2f(s[r] - mn); }
        const f32x8 lo = __builtin_shufflevector(s, s, 0, 1, 2, 3, 4, 5, 6, 7), hi = __builtin_shufflevector(s, s, 8, 9, 10, 11, 12, 13, 14, 15);
        const unsigned va = cb + CH_VT + tl * 1216 + h * 304 + vrow * 16;
        o = __builtin_amdgcn_smfmac_f32_32x32x32_bf16(ldsrd128(va), __builtin_bit_cast(bf16x16, lo), o, (int)0xDDDDDDDD, 0, 0);
        o = __builtin_amdgcn_smfmac_f32_32x32x32_bf16(ldsrd128(va + 608), __builtin_bit_cast(bf16x16, hi), o, (int)0xDDDDDDDD, 0, 0);
    }
    mrow = m;
}
__device__ __forceinline__ bool attn_range_ok(const f32x16& o) {
    const float l = o[10];
    bool ok = (l >= 5.9604645e-8f) && (l <= 1.8446744e19f);
#pragma unroll
    for (int r = 0; r < 10; ++r) ok = ok && (fabsf(o[r]) <= 3.0e38f);
    return __all(ok);
}
__device__ __forceinline__ void attn_dma(const char* ws, int head, int first, unsigned lds0, int w, int lane) {
    const char* src = ws + WS_KV + (size_t)head * HEAD_B + first * CH_B + lane * 16;
#pragma unroll
    for (int i = 0; i < 10; ++i) { int p = w + 8 * i; p = p > 73 ? 73 : p; glds16(src + p * 1024, lds0 + first * CH_B + p * 1024); }
}
struct AttnQ { bf16x8 fA, fB; s16x4 rA, rB; unsigned a, b; };
__device__ __forceinline__ void attn_loadq(AttnQ& Q, const char* ws, int head, int qtA, int lane) {
    const char* qp = ws + WS_Q + (size_t)head * QH_B + (size_t)qtA * QT_B;
    asm volatile("global_load_dwordx4 %0, %1, off sc1" : "=v"(Q.fA) : "v"(qp + lane * 16) : "memory");
    asm volatile("global_load_dwordx4 %0, %1, off sc1" : "=v"(Q.fB) : "v"(qp + QT_B + lane * 16) : "memory");
    asm volatile("global_load_dword %0, %1, off sc1" : "=v"(Q.a) : "v"(qp + 1024 + (lane & 31) * 4) : "memory");
    asm volatile("global_load_dword %0, %1, off sc1" : "=v"(Q.b) : "v"(qp + QT_B + 1024 + (lane & 31) * 4) : "memory");
}
#define ATTN_QWAITBAR(Q, N) asm volatile("s_waitcnt vmcnt(" #N ") lgkmcnt(0)\n\ts_barrier" : "+v"(Q.fA), "+v"(Q.fB), "+v"(Q.a), "+v"(Q.b) :: "memory")
__device__ __forceinline__ void attn_fixq(AttnQ& Q, int lane) {
    Q.rA = __builtin_bit_cast(s16x4, (u32x2){lane < 32 ? Q.a : 0u, 0u}); Q.rB = __builtin_bit_cast(s16x4, (u32x2){lane < 32 ? Q.b : 0u, 0u});
}

__device__ __forceinline__ void attn_loop(AttnQ& Q, const char* ws, int head, unsigned lds0, int w, int lane, f32x16& oA, f32x16& oB) {
    const int kpart = w >> 2, m = lane & 31, h = lane >> 5;
    const int vrow = (m < 19) ? m : ((m == 22) ? 18 : 0);
    unsigned ka = lds0 + kpart * CH_B + lane * 16, k2a = lds0 + kpart * CH_B + CH_K2 + m * 4, va = lds0 + kpart * CH_B + CH_VT + h * 304 + vrow * 16;
    unsigned idx = 0xDDDDDDDDu;
    int cnt = 16;
    const unsigned long long vmask = (((1ull << 19) - 1) | (1ull << 22)) * 0x100000001ull;
    oA = f32x16{}; oB = f32x16{};
    const char* src2 = ws + WS_KV + (size_t)head * HEAD_B + 2 * CH_B;
    unsigned lane16 = lane * 16, ldsb = lds0 + 2 * CH_B, pnext = w;
#define EXP4(d0, d1, d2, d3, s0, s1, s2, s3) \
        "v_exp_f32 v" #d0 ", v" #s0 "\n v_exp_f32 v" #d1 ", v" #s1 "\n v_exp_f32 v" #d2 ", v" #s2 "\n v_exp_f32 v" #d3 ", v" #s3 "\n"
#define VRD(x) "s_mov_b64 exec, %12\n" x "s_mov_b64 exec, -1\n"
#define INLOOP_DMA "s_cmp_lt_u32 %10, 12\n s_cbranch_scc1 3f\n" DMA2 DMA2 "3:\n"
#define MIDBAR "s_waitcnt vmcnt(0)\n s_barrier\n"
#define DMA2 "s_min_u32 s44, %11, 73\n s_lshl_b32 s44, s44, 10\n s_add_u32 m0, %14, s44\n v_add_u32 v126, s44, %13\n s_add_u32 %11, %11, 8\n global_load_lds_dwordx4 v126, %15\n"
#define KEYTILE(PK, OK, PK2, OK2, OVH, PV, OVL, PL, PH) \
        "v_mfma_f32_32x32x16_bf16 v[80:95], v[112:115], %3, 0\n" \
        PK "ds_read_b128 v[112:115], %7 offset:" #OK "\n" \
        PL EXP4(96, 97, 98, 99, 64, 65, 66, 67) PH \
        "v_mfma_f32_32x32x8_bf16 v[80:95], v[116:117], %5, v[80:95]\n" \
        PK2 "ds_read_b32 v116, %8 offset:" #OK2 "\n" \
        EXP4(100, 101, 102, 103, 68, 69, 70, 71) \
        "v_smfmac_f32_32x32x32_bf16 %1, v[122:125], v[104:111], %6\n" \
        VRD("ds_read_b128 v[122:125], %9 offset:" #OVH "\n") \
        EXP4(104, 105, 106, 107, 72, 73, 74, 75) \
        "s_waitcnt lgkmcnt(3)\n" \
        "v_smfmac_f32_32x32x32_bf16 %0, v[118:121], v[96:103], %6\n" \
        EXP4(108, 109, 110, 111, 76, 77, 78, 79) \
        "s_waitcnt lgkmcnt(2)\n" \
        "v_mfma_f32_32x32x16_bf16 v[64:79], v[112:115], %2, 0\n" \
        PL EXP4(96, 97, 98, 99, 80, 81, 82, 83) PH \
        "s_waitcnt lgkmcnt(1)\n" \
        "v_mfma_f32_32x32x8_bf16 v[64:79], v[116:117], %4, v[64:79]\n" \
        EXP4(100, 101, 102, 103, 84, 85, 86, 87) \
        "s_waitcnt lgkmcnt(0)\n" \
        "v_smfmac_f32_32x32x32_bf16 %0, v[122:125], v[104:111], %6\n" \
        EXP4(104, 105, 106, 107, 88, 89, 90, 91) \
        "v_smfmac_f32_32x32x32_bf16 %1, v[118:121], v[96:103], %6\n" \
        PV VRD("ds_read_b128 v[118:121], %9 offset:" #OVL "\n") \
        EXP4(108, 109, 110, 111, 92, 93, 94, 95)
#define LOOPASM(PL, PH, PRE) \
    asm volatile( \
        PRE \
        "ds_read_b128 v[112:115], %7\n" \
        "ds_read_b32 v116, %8\n" \
        "v_mov_b32 v117, 0\n" \
        "v_mov_b32 v104, 0\n v_mov_b32 v105, 0\n v_mov_b32 v106, 0\n v_mov_b32 v107, 0\n" \
        "v_mov_b32 v108, 0\n v_mov_b32 v109, 0\n v_mov_b32 v110, 0\n v_mov_b32 v111, 0\n" \
        "v_mov_b32 v122, 0\n v_mov_b32 v123, 0\n v_mov_b32 v124, 0\n v_mov_b32 v125, 0\n" \
        "v_mov_b32 v118, 0\n v_mov_b32 v119, 0\n v_mov_b32 v120, 0\n v_mov_b32 v121, 0\n" \
        "s_waitcnt lgkmcnt(0)\n" \
        "v_mfma_f32_32x32x16_bf16 v[64:79], v[112:115], %2, 0\n" \
        "v_mfma_f32_32x32x8_bf16 v[64:79], v[116:117], %4, v[64:79]\n" \
        VRD("ds_read_b128 v[118:121], %9\n") \
        "s_nop 7\n s_nop 7\n" \
        "s_mov_b32 s43, m0\n" \
        "1:\n" \
        INLOOP_DMA \
        "s_movk_i32 s40, 2048\n s_movk_i32 s41, 256\n s_movk_i32 s42, 2432\n" \
        "s_cmp_lg_u32 %10, 9\n s_cbranch_scc1 2f\n" \
        MIDBAR \
        "s_mov_b32 s40, 61440\n s_mov_b32 s41, 73984\n s_mov_b32 s42, 58752\n" \
        "2:\n" \
        KEYTILE("", 1024, "", 128, 608, "", 1216, PL, PH) \
        KEYTILE("v_add_u32 %7, s40, %7\n", 0, "v_add_u32 %8, s41, %8\n", 0, 1824, "v_add_u32 %9, s42, %9\n", 0, PL, PH) \
        "s_sub_u32 %10, %10, 1\n s_cmp_lg_u32 %10, 0\n s_cbranch_scc1 1b\n" \
        "s_waitcnt lgkmcnt(0)\n" \
        "v_smfmac_f32_32x32x32_bf16 %1, v[122:125], v[104:111], %6\n" \
        "s_mov_b32 m0, s43\n s_setprio 0\n s_nop 7\n s_nop 7\n" \
        : "+v"(oA), "+v"(oB), "+v"(Q.fA), "+v"(Q.fB), "+v"(Q.rA), "+v"(Q.rB), "+v"(idx), "+v"(ka), "+v"(k2a), "+v"(va), "+s"(cnt), "+s"(pnext) \
        : "s"(vmask), "v"(lane16), "s"(ldsb), "s"(src2) \
        : "v64", "v65", "v66", "v67", "v68", "v69", "v70", "v71", "v72", "v73", "v74", "v75", "v76", "v77", "v78", "v79", \
          "v80", "v81", "v82", "v83", "v84", "v85", "v86", "v87", "v88", "v89", "v90", "v91", "v92", "v93", "v94", "v95", \
          "v96", "v97", "v98", "v99", "v100", "v101", "v102", "v103", "v104", "v105", "v106", "v107", "v108", "v109", "v110", "v111", \
          "v112", "v113", "v114", "v115", "v116", "v117", "v118", "v119", "v120", "v121", "v122", "v123", "v124", "v125", \
          "v126", "s40", "s41", "s42", "s43", "s44", "scc", "memory")
    if (w < 4) { LOOPASM("s_setprio 0\n", "s_setprio 2\n", "s_setprio 2\n"); } else { LOOPASM("", "", "s_setprio 1\n"); }
#undef LOOPASM
#undef KEYTILE
#undef DMA2
#undef VRD
#undef EXP4
}
__device__ __forceinline__ void attn_finish(AttnQ& Q, f32x16& oA, f32x16& oB, rsrc_t rs, int head, int qtA, unsigned lds0, int w, int lane) {
    const int kpart = w >> 2, m = lane & 31, h = lane >> 5;
    float mA = 0.f, mB = 0.f;
    if (!attn_range_ok(oA)) attn_slow(lds0 + kpart * CH_B, lane, Q.fA, Q.rA, oA, mA);
    if (!attn_range_ok(oB)) attn_slow(lds0 + kpart * CH_B, lane, Q.fB, Q.rB, oB, mB);
    LBAR();
    {
        LAS float* xch = (LAS float*)(uintptr_t)(lds0 + w * 3072);
        const f32x16& give = kpart ? oA : oB;
#pragma unroll
        for (int r = 0; r < 11; ++r) xch[r * 64 + lane] = give[r];
        xch[11 * 64 + lane] = kpart ? mA : mB;
    }
    LBAR();
    {
        LAS const float* xin = (LAS const float*)(uintptr_t)(lds0 + (w ^ 4) * 3072);
        const f32x16& keep = kpart ? oB : oA;
        const float m0 = kpart ? mB : mA, m1 = xin[11 * 64 + lane];
        const float mm = fmaxf(m0, m1), f0 = __builtin_amdgcn_exp2f(m0 - mm), f1 = __builtin_amdgcn_exp2f(m1 - mm);
        float v[11];
#pragma unroll
        for (int r = 0; r < 11; ++r) v[r] = keep[r] * f0 + xin[r * 64 + lane] * f1;
        const float inv = 1.0f / v[10];
        const int tb = qtA + kpart;
        const u32x4 o8 = {cvtpk(v[0] * inv, v[1] * inv), cvtpk(v[2] * inv, v[3] * inv), cvtpk(v[4] * inv, v[5] * inv), cvtpk(v[6] * inv, v[7] * inv)};
        st16(rs, (unsigned)WS_OM + ((2 * tb + (m >> 4)) * 16 + (head >> 1)) * 1024 + ((2 * (head & 1) + h) * 16 + (m & 15)) * 16, o8);
        const unsigned rr = cvtpk(v[8] * inv, v[9] * inv);
        const unsigned r0 = __shfl(rr, (lane & 7) * 4), r1 = __shfl(rr, (lane & 7) * 4 + 1), r2 = __shfl(rr, (lane & 7) * 4 + 2), r3 = __shfl(rr, (lane & 7) * 4 + 3);
        if (lane < 8) st16(rs, (unsigned)WS_OR + (tb * 32 + head) * 128 + lane * 16, (u32x4){r0, r1, r2, r3});
    }
}

constexpr int P4_SLAB = 24576, P4_ZT = P4_SLAB + 576 * 144, P4_RED = P4_ZT + 72 * 144;
__device__ __forceinline__ void proj_dma(const u16* __restrict__ wo, int cg, unsigned lds0, int w, int lane) {
#pragma unroll
    for (int i = 0; i < 11; ++i) {
        int p = w + 8 * i; p = p > 80 ? 80 : p;
        const int ck = p * 64 + lane, row = ck / 9, c9 = ck - row * 9;
        glds16(wo + (size_t)row * D + cg * 72 + c9 * 8, lds0 + P4_SLAB + p * 1024);
    }
}
struct GateRegs { u32x4 z0, z1, q0, q1; };
__device__ __forceinline__ void gate_issue(GateRegs& R, rsrc_t rs, const u16* __restrict__ bq, int tb, int cg, int w, int lane) {
    const int tid = threadIdx.x;
    R.z0 = ld16(rs, (unsigned)WS_ZT + (unsigned)(cg * 72 + (tid >> 3)) * (S * 2) + (tb * 64 + (tid & 7) * 8) * 2);
    R.z1 = R.z0;
    if (tid < 64) R.z1 = ld16(rs, (unsigned)WS_ZT + (unsigned)(cg * 72 + 64 + (tid >> 3)) * (S * 2) + (tb * 64 + (tid & 7) * 8) * 2);
    const int G = lane >> 4;
    { const int u = 4 * w + G, tg = u / 9, cb = u - tg * 9; R.q0 = *(const u32x4*)(bq + (size_t)(tb * 64 + tg * 16 + (lane & 15)) * D + cg * 72 + cb * 8); }
    R.q1 = R.q0;
    if (w == 0) { const int u = 32 + G, tg = u / 9, cb = u - tg * 9; R.q1 = *(const u32x4*)(bq + (size_t)(tb * 64 + tg * 16 + (lane & 15)) * D + cg * 72 + cb * 8); }
}
__device__ __forceinline__ void gate_stage(const GateRegs& R, unsigned lds0) {
    const int tid = threadIdx.x;
    ldswr4(lds0 + P4_ZT + (tid >> 3) * 144 + (tid & 7) * 16, R.z0);
    if (tid < 64) ldswr4(lds0 + P4_ZT + (64 + (tid >> 3)) * 144 + (tid & 7) * 16, R.z1);
}
__device__ __forceinline__ void gate_unit(unsigned lds0, u16* __restrict__ out, const u32x4& qq, int u, int tb, int cg, int lane) {
    const int tg = u / 9, cb = u - tg * 9, q = (lane & 15) >> 2, p = lane & 3;
    const unsigned a = lds0 + P4_ZT + (8 * cb + q) * 144 + (16 * tg + 4 * p) * 2;
    const u32x2 zl = __builtin_bit_cast(u32x2, trread(a)), zh = __builtin_bit_cast(u32x2, trread(a + 4 * 144));
    const u32x4 r = {cvtpk(bflo(qq[0]) * bflo(zl[0]), bfhi(qq[0]) * bfhi(zl[0])), cvtpk(bflo(qq[1]) * bflo(zl[1]), bfhi(qq[1]) * bfhi(zl[1])),
                     cvtpk(bflo(qq[2]) * bflo(zh[0]), bfhi(qq[2]) * bfhi(zh[0])), cvtpk(bflo(qq[3]) * bflo(zh[1]), bfhi(qq[3]) * bfhi(zh[1]))};
    *(u32x4*)(out + (size_t)(tb * 64 + tg * 16 + (lane & 15)) * OUTW + D + cg * 72 + cb * 8) = r;
}
__device__ __forceinline__ void gate_finish(const GateRegs& R, unsigned lds0, u16* out, int tb, int cg, int w, int lane) {
    gate_unit(lds0, out, R.q0, 4 * w + (lane >> 4), tb, cg, lane);
    if (w == 0) gate_unit(lds0, out, R.q1, 32 + (lane >> 4), tb, cg, lane);
}
struct ProjW { bf16x8 f[9][5]; };
__device__ __forceinline__ void proj_readw(ProjW& W, unsigned lds0, int w, int lane) {
    const int kh = w >> 2, kq = lane >> 4, q = (lane & 15) >> 2, p = lane & 3;
#pragma unroll
    for (int s = 0; s < 9; ++s) {
        const int st = kh * 9 + s;
        int r0, r1;
        if (st < 16) { const int hd = 2 * st + (kq >> 1), hh = kq & 1; r0 = 18 * hd + 4 * hh + q; r1 = r0 + 8; }
        else { const int hd = 16 * (st - 16) + 4 * kq + (q >> 1); r0 = 18 * hd + 16 + (q & 1); r1 = r0 + 36; }
#pragma unroll
        for (int c = 0; c < 5; ++c) {
            const int cc = (c == 4 && p >= 2) ? 64 + (p & 1) * 4 : 16 * c + 4 * p;
            W.f[s][c] = cat8(trread(lds0 + P4_SLAB + r0 * 144 + cc * 2), trread(lds0 + P4_SLAB + r1 * 144 + cc * 2));
        }
    }
}
__device__ __forceinline__ void proj_tile(const ProjW& W, rsrc_t rs, u16* __restrict__ out, int tb64, int cg, unsigned lds0, int w, int lane) {
    const int tt = w & 3, kh = w >> 2, t16 = tb64 * 4 + tt, kq = lane >> 4;
    bf16x8 af[9];
#pragma unroll
    for (int s = 0; s < 9; ++s) {
        const int st = kh * 9 + s;
        if (st < 16) af[s] = __builtin_bit_cast(bf16x8, ld16(rs, (unsigned)WS_OM + (t16 * 16 + st) * 1024 + lane * 16));
        else {
            const unsigned ro = (unsigned)WS_OR + ((t16 >> 1) * 32 + 16 * (st - 16) + 4 * (lane >> 4)) * 128 + ((t16 & 1) * 16 + (lane & 15)) * 4;
            af[s] = __builtin_bit_cast(bf16x8, (u32x4){ld4(rs, ro), ld4(rs, ro + 128), ld4(rs, ro + 256), ld4(rs, ro + 384)});
        }
    }
    f32x4 acc[5] = {};
#pragma unroll
    for (int s = 0; s < 9; ++s)
#pragma unroll
        for (int c = 0; c < 5; ++c) acc[c] = MFMA16(W.f[s][c], af[s], acc[c]);
    LAS f32x4* red = (LAS f32x4*)(uintptr_t)(lds0 + P4_RED + tt * 5120);
    if (kh == 1) {
#pragma unroll
        for (int c = 0; c < 5; ++c) red[c * 64 + lane] = acc[c];
    }
    LBAR();
    if (kh == 0) {
        u16* op = out + (size_t)(t16 * 16 + (lane & 15)) * OUTW + cg * 72 + 4 * kq;
#pragma unroll
        for (int c = 0; c < 5; ++c) {
            const f32x4 v = acc[c] + red[c * 64 + lane];
            if (c < 4 || kq < 2) *(u32x2*)(op + 16 * c) = (u32x2){cvtpk(v[0], v[1]), cvtpk(v[2], v[3])};
        }
    }
}

__device__ __forceinline__ void conv_phase(const u16* ssm_k, const u16* ssm_D, char* ws, rsrc_t rs, int pair, bool first, bool pubqkv, bool last,
                                           AttnQ& Q, int head, int qtA, unsigned epoch, unsigned lds0, int w, int lane STAMP_ARG) {
    const int grp = w >> 2, c = 2 * pair + grp;
    const unsigned base = lds0 + CV_BASE + grp * CV_B;
    ConvRegs R;
    conv_issue_h(R, ssm_k, c);
    if (first) { if (w == 0) poll_flags(rs, FL_KV + (pair >> 5) * 128, 32, epoch, lane); LBAR(); }
    conv_issue_u(R, rs, c);
    conv_build(R, base);
    conv_build_u(R, base);
    WAITBAR(0);
    if (pubqkv) publish(rs, FL_QKV + blockIdx.x * 4, epoch);
    STAMP(1, 3);
    f32x16 d0, d1;
    conv_main(base, w, lane, d0, d1, last, rs, epoch);
    STAMP(1, 4);
    WAITBAR(0);
    if (last) { attn_loadq(Q, ws, head, qtA, lane); attn_dma(ws, head, 0, lds0, w, lane); }
    conv_partials(base, w & 3, lane, d0, d1);
    LBAR();
    conv_finish(base, rs, ssm_D, c, R.u);
}

__global__ __launch_bounds__(512, 2) void fused(const u16* __restrict__ x, const u16* __restrict__ wqkv, const u16* __restrict__ wo,
                                                const u16* __restrict__ bq, const u16* __restrict__ bk, const u16* __restrict__ bv,
                                                const u16* __restrict__ shift_k, const u16* __restrict__ ssm_k, const u16* __restrict__ ssm_D,
                                                char* __restrict__ ws, u16* __restrict__ out) {
    extern __shared__ __attribute__((aligned(16))) char lds[];
    const unsigned lds0 = (unsigned)(uintptr_t)lds;
    const int lane = threadIdx.x & 63, w = __builtin_amdgcn_readfirstlane(threadIdx.x >> 6), b = blockIdx.x;
    const rsrc_t rs = ws_rsrc(ws);
    STAMP_DECL;
    STAMP(0, 0);
    const unsigned epoch = *(const __attribute__((address_space(4))) unsigned*)(uintptr_t)ws + 1u;
    phase1(x, wqkv, bk, bv, shift_k, ws, rs, lds0, w, lane, epoch STAMP_PASS);
    STAMP(0, 2);
    const int head = (b & 7) * 4 + ((b >> 3) & 3), qb = b >> 5, qtA = qb * 8 + (w & 3) * 2;
    AttnQ Q;
    if (b < 224) conv_phase(ssm_k, ssm_D, ws, rs, b, true, true, true, Q, head, qtA, epoch, lds0, w, lane STAMP_PASS);
    else {
        conv_phase(ssm_k, ssm_D, ws, rs, 224 + 2 * (b - 224), true, false, false, Q, head, qtA, epoch, lds0, w, lane STAMP_PASS);
        LBAR();
        conv_phase(ssm_k, ssm_D, ws, rs, 225 + 2 * (b - 224), true, false, true, Q, head, qtA, epoch, lds0, w, lane STAMP_PASS);
    }
    ATTN_QWAITBAR(Q, 0);
    publish(rs, FL_Z + b * 4, epoch);
    attn_fixq(Q, lane);
    STAMP(1, 5);
    f32x16 oA, oB;
    attn_loop(Q, ws, head, lds0, w, lane, oA, oB);
    STAMP(1, 6);
    attn_finish(Q, oA, oB, rs, head, qtA, lds0, w, lane);
    const int i5 = b & 31, ptb = (b >> 5) * 4 + ((i5 & 7) >> 1), pcg = (i5 & 1) * 4 + (i5 >> 3);
    proj_dma(wo, pcg, lds0, w, lane);
    if (w == 1) poll_flags(rs, FL_Z, 256, epoch, lane);
    WAITBAR(11);
    publish(rs, FL_O + b * 4, epoch);
    if (b == 0 && threadIdx.x == 0) *(unsigned*)ws = epoch;
    GateRegs G;
    gate_issue(G, rs, bq, b >> 3, b & 7, w, lane);
    gate_stage(G, lds0);
    WAITBAR(0);
    gate_finish(G, lds0, out, b >> 3, b & 7, w, lane);
    ProjW W;
    proj_readw(W, lds0, w, lane);
    if (w == 0) poll_flags(rs, FL_O + (b >> 5) * 128, 32, epoch, lane);
    LBAR();
    proj_tile(W, rs, out, ptb, pcg, lds0, w, lane);
    STAMP(1, 7);
    STAMP_FLUSH(0);
}

extern "C" void kernel_launch(void* const* d_in, const int* in_sizes, int n_in, void* d_out, int out_size, void* d_ws, size_t ws_size,
                              hipStream_t stream) {
    (void)hipFuncSetAttribute((const void*)fused, hipFuncAttributeMaxDynamicSharedMemorySize, LDS_BYTES);
    fused<<<256, 512, LDS_BYTES, stream>>>((const u16*)d_in[0], (const u16*)d_in[1], (const u16*)d_in[2], (const u16*)d_in[3], (const u16*)d_in[4],
                                           (const u16*)d_in[5], (const u16*)d_in[6], (const u16*)d_in[7], (const u16*)d_in[8], (char*)d_ws, (u16*)d_out);
}
```

```cpp
#include <hip/hip_runtime.h>
#include <hip/hip_bf16.h>
#include <stdint.h>

#define LAS __attribute__((address_space(3)))
typedef __attribute__((ext_vector_type(8))) __bf16 bf16x8;
typedef __attribute__((ext_vector_type(16))) __bf16 bf16x16;
typedef __attribute__((ext_vector_type(4))) short s16x4;
typedef __attribute__((ext_vector_type(8))) short s16x8;
typedef __attribute__((ext_vector_type(16))) float f32x16;
typedef __attribute__((ext_vector_type(8))) float f32x8;
typedef __attribute__((ext_vector_type(4))) float f32x4;
typedef __attribute__((ext_vector_type(4))) unsigned u32x4;
typedef __attribute__((ext_vector_type(2))) unsigned u32x2;
typedef unsigned short u16;

constexpr int S = 2048, D = 576, NH = 32;
constexpr int OUTW = 2 * D;
constexpr int CH_K2 = 16384, CH_VT = 18432, CH_B = 37888, HEAD_B = 4 * CH_B;
constexpr int QT_B = 1152, QH_B = 64 * QT_B;
constexpr size_t WS_FLAG = 0;
constexpr unsigned FL_GEN = 0, FL_KV = 1024, FL_QKV = 2560, FL_Z = 3584, FL_O = 4608;
constexpr size_t WS_KV = 8192;
constexpr size_t WS_Q = WS_KV + (size_t)NH * HEAD_B;
constexpr size_t WS_OM = WS_Q + (size_t)NH * QH_B;
constexpr size_t WS_OR = WS_OM + (size_t)128 * 16 * 1024;
constexpr size_t WS_KVT = WS_OR + (size_t)64 * 32 * 128;
constexpr size_t WS_ZT = WS_KVT + (size_t)S * D * 2;
constexpr size_t WS_END = WS_ZT + (size_t)S * D * 2;
constexpr float QSCALE = 0.23570226039551584f * 1.4426950408889634f;
constexpr int LDS_BYTES = 158720;

#define STAMP_DECL
#define STAMP(kid, pt)
#define STAMP_FLUSH(kid)
#define STAMP_ARG
#define STAMP_PASS
#define SBAR() __builtin_amdgcn_sched_barrier(0)
#define MFMA32(a, b, c) __builtin_amdgcn_mfma_f32_32x32x16_bf16(a, b, c, 0, 0, 0)
#define MFMA16(a, b, c) __builtin_amdgcn_mfma_f32_16x16x32_bf16(a, b, c, 0, 0, 0)
#define WAITBAR(V) asm volatile("s_waitcnt vmcnt(" #V ") lgkmcnt(0)\n\ts_barrier" ::: "memory")
#define LBAR() asm volatile("s_waitcnt lgkmcnt(0)\n\ts_barrier" ::: "memory")

__device__ __forceinline__ void glds16(const void* g, unsigned lds_base) {
    unsigned sv;
    asm volatile("s_mov_b32 %0, m0\n\ts_mov_b32 m0, %2\n\ts_nop 0\n\tglobal_load_lds_dwordx4 %1, off\n\ts_mov_b32 m0, %0"
                 : "=&s"(sv) : "v"(g), "s"(lds_base) : "memory");
}
__device__ __forceinline__ unsigned cvtpk(float lo, float hi) {
    typedef __attribute__((ext_vector_type(2))) float f2; typedef __attribute__((ext_vector_type(2))) __bf16 b2;
    f2 v = {lo, hi};
    return __builtin_bit_cast(unsigned, __builtin_convertvector(v, b2));
}
__device__ __forceinline__ float bflo(unsigned u) { return __uint_as_float(u << 16); }
__device__ __forceinline__ float bfhi(unsigned u) { return __uint_as_float(u & 0xffff0000u); }
__device__ __forceinline__ float rbf(float x) { return bflo(cvtpk(x, 0.f)); }
__device__ __forceinline__ s16x4 trread(unsigned a) {
    return __builtin_bit_cast(s16x4, __builtin_amdgcn_ds_read_tr16_b64_v4i16((LAS s16x4*)(uintptr_t)a));
}
__device__ __forceinline__ bf16x8 cat8(s16x4 a, s16x4 b) { return __builtin_bit_cast(bf16x8, (s16x8)__builtin_shufflevector(a, b, 0, 1, 2, 3, 4, 5, 6, 7)); }
__device__ __forceinline__ bf16x8 ldsrd128(unsigned a) { return *(LAS const bf16x8*)(uintptr_t)a; }
__device__ __forceinline__ u32x4 ldsrdu4(unsigned a) { return *(LAS const u32x4*)(uintptr_t)a; }
__device__ __forceinline__ u32x2 ldsrdu2(unsigned a) { return *(LAS const u32x2*)(uintptr_t)a; }
__device__ __forceinline__ void ldswr4(unsigned a, u32x4 v) { *(LAS u32x4*)(uintptr_t)a = v; }
__device__ __forceinline__ void ldswr2(unsigned a, u32x2 v) { *(LAS u32x2*)(uintptr_t)a = v; }
__device__ __forceinline__ void ldswr1(unsigned a, unsigned v) { *(LAS unsigned*)(uintptr_t)a = v; }

__device__ __forceinline__ unsigned l2touch(const void* p) { unsigned r; asm volatile("global_load_dword %0, %1, off" : "=v"(r) : "v"(p) : "memory"); return r; }
#define KEEP(x) asm volatile("" :: "v"(x))

typedef __amdgpu_buffer_rsrc_t rsrc_t;
__device__ __forceinline__ rsrc_t ws_rsrc(char* ws) { return __builtin_amdgcn_make_buffer_rsrc(ws, 0, (int)WS_END, 0x00020000); }
__device__ __forceinline__ void st16(rsrc_t r, unsigned off, u32x4 v) { __builtin_amdgcn_raw_buffer_store_b128(v, r, off, 0, 16); }
__device__ __forceinline__ void st8(rsrc_t r, unsigned off, u32x2 v) { __builtin_amdgcn_raw_buffer_store_b64(v, r, off, 0, 16); }
__device__ __forceinline__ void st4(rsrc_t r, unsigned off, unsigned v) { __builtin_amdgcn_raw_buffer_store_b32(v, r, off, 0, 16); }
__device__ __forceinline__ u32x4 ld16(rsrc_t r, unsigned off) { return __builtin_amdgcn_raw_buffer_load_b128(r, off, 0, 16); }
__device__ __forceinline__ unsigned ld4(rsrc_t r, unsigned off) { return __builtin_amdgcn_raw_buffer_load_b32(r, off, 0, 16); }

__device__ __forceinline__ void publish(rsrc_t rs, unsigned off, unsigned epoch) { if (threadIdx.x == 0) st4(rs, off, epoch); }
__device__ __forceinline__ void poll_flags(rsrc_t rs, unsigned off, int nwords, unsigned epoch, int lane) {
    for (int spin = 0; spin < (1 << 18); ++spin) {
        bool ok = true;
        if (lane * 4 < nwords) { const u32x4 v = ld16(rs, off + lane * 16); ok = v[0] == epoch && v[1] == epoch && v[2] == epoch && v[3] == epoch; }
        if (__all(ok)) break;
        __builtin_amdgcn_s_sleep(4);
        asm volatile("" ::: "memory");
    }
    asm volatile("" ::: "memory");
}

constexpr int KVS_K = 0, KVS_W = 67 * 144, KVS_T = KVS_W + 512, KVS_B = KVS_T + 64 * 144;
struct KvRegs { u32x4 k, v, x; };
__device__ __forceinline__ u32x4 gload16(const void* p) {
    u32x4 r; asm volatile("global_load_dwordx4 %0, %1, off" : "=v"(r) : "v"(p) : "memory"); return r;
}
__device__ __forceinline__ void kv_issue(KvRegs& R, const u16* __restrict__ bk, const u16* __restrict__ bv, const u16* __restrict__ shift_k, int tb, int cg) {
    const int tid = threadIdx.x, t = tb * 64 + (tid >> 3), c = cg * 64 + (tid & 7) * 8;
    R.k = gload16(bk + (size_t)t * D + c);
    R.v = gload16(bv + (size_t)t * D + c);
    const u16* xp = bk + c;
    if (tid < 24) { const int th = tb * 64 - 3 + (tid >> 3); xp = bk + (size_t)(th < 0 ? 0 : th) * D + c; }
    else if (tid >= 64 && tid < 96) xp = shift_k + cg * 256 + (tid - 64) * 8;
    R.x = gload16(xp);
}
#define KV_WAIT(R, N) asm volatile("s_waitcnt vmcnt(" #N ")" : "+v"(R.k), "+v"(R.v), "+v"(R.x) :: "memory")
__device__ __forceinline__ void kv_stage(const KvRegs& R, int tb, unsigned sc) {
    const int tid = threadIdx.x;
    ldswr4(sc + KVS_K + ((tid >> 3) + 3) * 144 + (tid & 7) * 16, R.k);
    if (tid < 24) ldswr4(sc + KVS_K + (tid >> 3) * 144 + (tid & 7) * 16, (tb * 64 - 3 + (tid >> 3) < 0) ? (u32x4){0u, 0u, 0u, 0u} : R.x);
    else if (tid >= 64 && tid < 96) ldswr4(sc + KVS_W + (tid - 64) * 16, R.x);
}
__device__ __forceinline__ float dot2bf(unsigned a, unsigned b, float c) {
    typedef __attribute__((ext_vector_type(2))) __bf16 b2;
    return __builtin_amdgcn_fdot2_f32_bf16(__builtin_bit_cast(b2, a), __builtin_bit_cast(b2, b), c, false);
}
__device__ __forceinline__ unsigned kv_pair(unsigned k0, unsigned k1, unsigned k2, unsigned k3, unsigned vv, unsigned wa0, unsigned wa1, unsigned wb0, unsigned wb1) {
    const float sl = dot2bf(__builtin_amdgcn_perm(k2, k3, 0x05040100u), wa0, dot2bf(__builtin_amdgcn_perm(k0, k1, 0x05040100u), wa1, 0.f));
    const float sh = dot2bf(__builtin_amdgcn_perm(k2, k3, 0x07060302u), wb0, dot2bf(__builtin_amdgcn_perm(k0, k1, 0x07060302u), wb1, 0.f));
    const unsigned s = cvtpk(sl, sh);
    return cvtpk(bflo(s) * bflo(vv), bfhi(s) * bfhi(vv));
}
__device__ __forceinline__ void kv_compute(const KvRegs& R, unsigned sc) {
    const int tid = threadIdx.x, trow = tid >> 3, ch = tid & 7;
    const unsigned ka = sc + KVS_K + trow * 144 + ch * 16;
    const u32x4 k0 = ldsrdu4(ka), k1 = ldsrdu4(ka + 144), k2 = ldsrdu4(ka + 288);
    const unsigned wa = sc + KVS_W + ch * 64;
    const u32x4 w0 = ldsrdu4(wa), w1 = ldsrdu4(wa + 16), w2 = ldsrdu4(wa + 32), w3 = ldsrdu4(wa + 48);
    const u32x4 r = {kv_pair(k0[0], k1[0], k2[0], R.k[0], R.v[0], w0[0], w0[1], w0[2], w0[3]),
                     kv_pair(k0[1], k1[1], k2[1], R.k[1], R.v[1], w1[0], w1[1], w1[2], w1[3]),
                     kv_pair(k0[2], k1[2], k2[2], R.k[2], R.v[2], w2[0], w2[1], w2[2], w2[3]),
                     kv_pair(k0[3], k1[3], k2[3], R.k[3], R.v[3], w3[0], w3[1], w3[2], w3[3])};
    const int row = (trow & ~7) | (7 - (trow & 7));
    ldswr4(sc + KVS_T + row * 144 + ch * 16, r);
}
__device__ __forceinline__ void kv_store(rsrc_t rs, int tb, int cg, unsigned sc, int w, int lane) {
    const int G = lane >> 4, q = (lane & 15) >> 2, p = lane & 3;
    const int cw = (w >> 1) * 16;
    s16x4 a[4];
#pragma unroll
    for (int u = 0; u < 4; ++u) a[u] = trread(sc + KVS_T + (16 * G + 4 * u + q) * 144 + (cw + 4 * p) * 2);
    const int i = lane & 15;
    if ((i >> 3) == (w & 1)) {
        const unsigned off = (unsigned)WS_KVT + (unsigned)(cg * 64 + cw + i) * (S * 2) + (tb * 64 + 16 * G) * 2;
        st16(rs, off, __builtin_bit_cast(u32x4, (s16x8)__builtin_shufflevector(a[0], a[1], 0, 1, 2, 3, 4, 5, 6, 7)));
        st16(rs, off + 16, __builtin_bit_cast(u32x4, (s16x8)__builtin_shufflevector(a[2], a[3], 0, 1, 2, 3, 4, 5, 6, 7)));
    }
}

#define ATEXP 0
#define NOKV 0
#define K1PF 0
template <bool B> struct BoolC { static constexpr bool value = B; };
constexpr int G1_STAGE = 32768, G1_SC = 4 * G1_STAGE;

__device__ __forceinline__ void qkv_tile(const bool ISV, const u16* __restrict__ x, const u16* __restrict__ wqkv, char* __restrict__ ws, rsrc_t rs,
                                         int mb, int ng, unsigned lds0, int w, int lane,
                                         const u16* __restrict__ bk, const u16* __restrict__ bv, const u16* __restrict__ shift_k, int kvtb, int kvcg, unsigned epoch STAMP_ARG) {
    const int ctl = w & 3, tb0 = (w >> 2) * 2;
    const bool active = (ng * 4 + ctl) < 54;
    const u16* xsrc[2]; const u16* wsrc[2];
#pragma unroll
    for (int i = 0; i < 2; ++i) {
        const int p = w * 2 + i;
        const int xr = 8 * p + (lane >> 3), xc = (lane & 7) ^ ((xr >> 1) & 7);
        xsrc[i] = x + (size_t)(mb * 128 + xr) * D + xc * 8;
        const int wr = 4 * p + (lane >> 4), wc = (lane & 15) ^ (((wr & 3) << 2) | ((wr >> 2) & 3));
        int col = ng * 128 + wc * 8; col = col > 1720 ? 1720 : col;
        wsrc[i] = wqkv + (size_t)wr * (3 * D) + col;
    }
    auto issue = [&](unsigned slot) {
#pragma unroll
        for (int i = 0; i < 2; ++i) {
            glds16(xsrc[i], lds0 + slot + (w * 2 + i) * 1024); xsrc[i] += 64;
            glds16(wsrc[i], lds0 + slot + 16384 + (w * 2 + i) * 1024); wsrc[i] += 64 * (3 * D);
        }
    };
    const int r = lane & 31, h = lane >> 5;
    unsigned xa[4];
#pragma unroll
    for (int s = 0; s < 4; ++s) xa[s] = lds0 + (tb0 * 32 + r) * 128 + (((2 * s + h) ^ ((r >> 1) & 7)) << 4);
    unsigned wa[2];
    {   const int blk = (lane >> 4) & 1, q = (lane & 15) >> 2, p = lane & 3;
#pragma unroll
        for (int u = 0; u < 2; ++u) {
            const int row = 8 * h + 4 * u + q, ch = 4 * ctl + 2 * blk + (p >> 1);
            wa[u] = lds0 + 16384 + row * 256 + ((ch ^ (((row & 3) << 2) | ((row >> 2) & 3))) << 4) + 8 * (p & 1);
        } }
    f32x16 acc0 = {}, acc1 = {};
    KvRegs R;
    issue(0);
    kv_issue(R, bk, bv, shift_k, kvtb, kvcg);
    issue(G1_STAGE);
    struct Frags { bf16x8 w[4], x0[4], x1[4]; };
    Frags fa, fb;
    auto rd = [&](Frags& f, unsigned st, int s) {
        f.w[s] = cat8(trread(wa[0] + st + s * 4096), trread(wa[1] + st + s * 4096));
        f.x0[s] = ldsrd128(xa[s] + st); f.x1[s] = ldsrd128(xa[s] + st + 4096);
    };
    const unsigned sc = lds0 + G1_SC;
    KV_WAIT(R, 4);
    kv_stage(R, kvtb, sc);
    WAITBAR(4);
    if (active) { _Pragma("unroll") for (int s = 0; s < 4; ++s) rd(fa, 0, s); }
    issue(2 * G1_STAGE);
    kv_compute(R, sc);
    auto kloop = [&](auto isv) {
        constexpr bool V = decltype(isv)::value;
        auto body = [&](const Frags& cur, Frags& nxt, unsigned st) {
            if (active) {
#pragma unroll
                for (int s = 0; s < 4; ++s) {
                    rd(nxt, st, s); SBAR();
                    if (V) { acc0 = MFMA32(cur.x0[s], cur.w[s], acc0); acc1 = MFMA32(cur.x1[s], cur.w[s], acc1); }
                    else   { acc0 = MFMA32(cur.w[s], cur.x0[s], acc0); acc1 = MFMA32(cur.w[s], cur.x1[s], acc1); }
                    SBAR();
                }
            }
        };
        int c = 0;
        asm volatile("" : "+s"(c));
#pragma unroll 1
        for (; c < 8; c += 2) {
            if (c == 0 || c == 6) WAITBAR(4); else if (c == 2) WAITBAR(10); else WAITBAR(8);
            if (c == 0) issue(3 * G1_STAGE);
            if (c + 4 < 9) issue((c & 3) * G1_STAGE);
            SBAR();
            body(fa, fb, ((c + 1) & 3) * G1_STAGE);
            if (c == 0) kv_store(rs, kvtb, kvcg, sc, w, lane);
            if (c == 0) WAITBAR(10); else if (c == 6) WAITBAR(0); else WAITBAR(8);
            if (c == 2) publish(rs, FL_KV + (kvcg * 32 + kvtb) * 4, epoch);
            if (c + 5 < 9) issue(((c + 1) & 3) * G1_STAGE);
            SBAR();
            body(fb, fa, ((c + 2) & 3) * G1_STAGE);
        }
        if (active) {
#pragma unroll
            for (int s = 0; s < 4; ++s) {
                if (V) { acc0 = MFMA32(fa.x0[s], fa.w[s], acc0); acc1 = MFMA32(fa.x1[s], fa.w[s], acc1); }
                else   { acc0 = MFMA32(fa.w[s], fa.x0[s], acc0); acc1 = MFMA32(fa.w[s], fa.x1[s], acc1); }
            }
        }
    };
    if (ISV) kloop(BoolC<true>{}); else kloop(BoolC<false>{});
    STAMP(0, 1);
    const int ct = ng * 4 + ctl;
    if (ISV) {
        if (active) {
#pragma unroll
            for (int i = 0; i < 2; ++i) {
                const f32x16& acc = i ? acc1 : acc0;
                const int T = mb * 4 + tb0 + i;
                const int col = (ct - 36) * 32 + r, head = col / 18, d = col - head * 18;
                const unsigned vp = (unsigned)WS_KV + head * HEAD_B + (((T >> 4) & 1) * 2 + (T >> 5)) * CH_B + CH_VT + (2 * (T & 15)) * 608 + h * 304 + d * 16;
#pragma unroll
                for (int g = 0; g < 2; ++g) {
                    const u32x4 o = {cvtpk(acc[8 * g], acc[8 * g + 1]), cvtpk(acc[8 * g + 2], acc[8 * g + 3]),
                                     cvtpk(acc[8 * g + 4], acc[8 * g + 5]), cvtpk(acc[8 * g + 6], acc[8 * g + 7])};
                    st16(rs, vp + g * 608, o);
                    if (d == 17) st16(rs, vp + g * 608 + 16, (u32x4){0x3f803f80u, 0x3f803f80u, 0x3f803f80u, 0x3f803f80u});
                }
            }
        }
    } else {
        LBAR();
        if (active) {
            const bool isq = ct < 18;
            const float scl = isq ? QSCALE : 1.0f;
            const int row = isq ? r : ((r & 16) | ((r & 4) << 1) | ((r & 8) >> 1) | (r & 3));
#pragma unroll
            for (int i = 0; i < 2; ++i) {
                const f32x16& acc = i ? acc1 : acc0;
#pragma unroll
                for (int g = 0; g < 4; ++g)
                    ldswr2(lds0 + (tb0 + i) * 8192 + (8 * ctl + 2 * g + h) * 256 + row * 8,
                           (u32x2){cvtpk(acc[4 * g] * scl, acc[4 * g + 1] * scl), cvtpk(acc[4 * g + 2] * scl, acc[4 * g + 3] * scl)});
            }
        }
        LBAR();
        const int g0 = ng * 32;
#pragma unroll
        for (int k5 = 0; k5 < 5; ++k5) {
            const int it = w + 8 * k5, tt = it / 10, cand = (it % 10) * 2 + h;
            const int blk0 = g0 / 9, blk = blk0 + (cand >> 2), cpos = cand & 3;
            const int pos = (cpos & 1) * 2 + (cpos >> 1) * 5;
            const int gq = blk * 9 + pos, lq = gq - g0;
            const bool in0 = lq >= 0 && lq < 32 && gq < 288, in1 = lq + 1 >= 0 && lq + 1 < 32 && gq + 1 < 288;
            if (in0 || in1) {
                const bool isq = gq < 144;
                const int pb = isq ? blk : blk - 16, head = 2 * pb + (cpos >> 1), hp = cpos & 1;
                const int T = mb * 4 + tt;
                const unsigned dst = isq ? (unsigned)WS_Q + head * QH_B + T * QT_B + hp * 512 + r * 16
                                         : (unsigned)WS_KV + head * HEAD_B + (((T >> 4) & 1) * 2 + (T >> 5)) * CH_B + (T & 15) * 1024 + hp * 512 + r * 16;
                const unsigned sa = lds0 + tt * 8192 + lq * 256 + r * 8;
                if (in0 && in1) { const u32x2 a = ldsrdu2(sa), b = ldsrdu2(sa + 256); st16(rs, dst, (u32x4){a[0], a[1], b[0], b[1]}); }
                else if (in0) st8(rs, dst, ldsrdu2(sa));
                else st8(rs, dst + 8, ldsrdu2(sa + 256));
            }
        }
        {
            const int blk = g0 / 9 + w, gq = blk * 9 + 4, lq = gq - g0;
            if (w < 5 && lq >= 0 && lq < 32 && gq < 288) {
                const int tt = lane >> 4, par = (lane >> 3) & 1, rq = lane & 7;
                const bool isq = gq < 144;
                const int head = 2 * (isq ? blk : blk - 16) + par, T = mb * 4 + tt;
                const unsigned sa = lds0 + tt * 8192 + lq * 256 + rq * 32 + par * 4;
                const u32x4 v = {*(LAS const unsigned*)(uintptr_t)sa, *(LAS const unsigned*)(uintptr_t)(sa + 8),
                                 *(LAS const unsigned*)(uintptr_t)(sa + 16), *(LAS const unsigned*)(uintptr_t)(sa + 24)};
                const unsigned dst = isq ? (unsigned)WS_Q + head * QH_B + T * QT_B + 1024 + rq * 16
                                         : (unsigned)WS_KV + head * HEAD_B + (((T >> 4) & 1) * 2 + (T >> 5)) * CH_B + CH_K2 + (T & 15) * 128 + rq * 16;
                st16(rs, dst, v);
            }
        }
    }
}

__device__ __forceinline__ void phase1(const u16* x, const u16* wqkv, const u16* bk, const u16* bv, const u16* shift_k, char* ws, rsrc_t rs,
                                       unsigned lds0, int w, int lane, unsigned epoch STAMP_ARG) {
    const int b = blockIdx.x;
    if (b >= 224) {
        const int t = 224 + (b - 224) * 2;
        KvRegs R0, R1;
        kv_issue(R0, bk, bv, shift_k, t & 31, t >> 5); kv_issue(R1, bk, bv, shift_k, (t + 1) & 31, (t + 1) >> 5);
        KV_WAIT(R0, 3); kv_stage(R0, t & 31, lds0);
        KV_WAIT(R1, 0); kv_stage(R1, (t + 1) & 31, lds0 + KVS_B);
        LBAR();
        kv_compute(R0, lds0); kv_compute(R1, lds0 + KVS_B);
        LBAR();
        kv_store(rs, t & 31, t >> 5, lds0, w, lane); kv_store(rs, (t + 1) & 31, (t + 1) >> 5, lds0 + KVS_B, w, lane);
        WAITBAR(0);
        publish(rs, FL_KV + t * 4, epoch);
        if (threadIdx.x == 0) st4(rs, FL_KV + (t + 1) * 4, epoch);
        return;
    }
    const int xcd = b & 7, i = b >> 3, mb = (xcd >> 1) * 4 + (i & 3), ng = (xcd & 1) * 7 + (i >> 2);
    qkv_tile(ng >= 9, x, wqkv, ws, rs, mb, ng, lds0, w, lane, bk, bv, shift_k, b & 31, b >> 5, epoch STAMP_PASS);
}

constexpr int CV_U = 33280, CV_B = CV_U + 7600;
constexpr int CV_BASE = LDS_BYTES - 2 * CV_B;

struct ConvRegs { u32x4 h0, h1; unsigned h2; u32x4 u; unsigned e[8]; };
__device__ __forceinline__ void conv_issue_h(ConvRegs& R, const u16* __restrict__ ssm_k, int c) {
    const int i = threadIdx.x & 255;
    const rsrc_t hr = __builtin_amdgcn_make_buffer_rsrc((void*)(ssm_k + (size_t)c * S), 0, S * 2, 0x00020000);
    const unsigned off = i < 4 ? 0u : (unsigned)(((9 * i - 32) >> 1) * 4);
    if (i < 64) {
#pragma unroll
        for (int j = 0; j < 8; ++j) R.e[j] = __builtin_amdgcn_raw_buffer_load_b16(hr, (unsigned)((i - 32 + j) * 2), 0, 0);
    }
    R.h0 = __builtin_amdgcn_raw_buffer_load_b128(hr, off, 0, 0);
    R.h1 = __builtin_amdgcn_raw_buffer_load_b128(hr, off + 16, 0, 0);
    R.h2 = __builtin_amdgcn_raw_buffer_load_b32(hr, off + 32, 0, 0);
}
__device__ __forceinline__ void conv_issue_u(ConvRegs& R, rsrc_t rs, int c) { R.u = ld16(rs, (unsigned)WS_KVT + (unsigned)c * (S * 2) + (threadIdx.x & 255) * 16); }
__device__ __forceinline__ void conv_build(const ConvRegs& R, unsigned base) {
    const int i = threadIdx.x & 255;
    if (i < 36) ldswr4(base + i * 16, (u32x4){R.e[0] | (R.e[1] << 16), R.e[2] | (R.e[3] << 16), R.e[4] | (R.e[5] << 16), R.e[6] | (R.e[7] << 16)});
    if (i >= 4 && i < 232) {
        const unsigned sh = (i & 1) * 16;
        const unsigned dw[9] = {R.h0[0], R.h0[1], R.h0[2], R.h0[3], R.h1[0], R.h1[1], R.h1[2], R.h1[3], R.h2};
        unsigned A[8], Bq[7];
#pragma unroll
        for (int n = 0; n < 8; ++n) A[n] = __builtin_amdgcn_alignbit(dw[n + 1], dw[n], sh);
#pragma unroll
        for (int n = 0; n < 7; ++n) Bq[n] = __builtin_amdgcn_alignbit(A[n + 1], A[n], 16);
        const unsigned a = base + 9 * i * 16;
#pragma unroll
        for (int u = 0; u < 9; ++u) {
            if (i < 231 || u < 1) {
                if (u & 1) ldswr4(a + u * 16, (u32x4){Bq[u >> 1], Bq[(u >> 1) + 1], Bq[(u >> 1) + 2], Bq[(u >> 1) + 3]});
                else ldswr4(a + u * 16, (u32x4){A[u >> 1], A[(u >> 1) + 1], A[(u >> 1) + 2], A[(u >> 1) + 3]});
            }
        }
    }
    if (i < 155) ldswr4(base + CV_U + i * 16, (u32x4){0u, 0u, 0u, 0u});
}
__device__ __forceinline__ void conv_build_u(const ConvRegs& R, unsigned base) {
    const int i = threadIdx.x & 255;
    ldswr4(base + CV_U + (31 + (i >> 2)) * 80 + (i & 3) * 16, R.u);
}
template <bool TWO>
__device__ __forceinline__ void conv_range(unsigned aaddr, unsigned baddr, int jb, int je, f32x16& d0, f32x16& d1) {
    unsigned aa = aaddr + jb * 256, bb = baddr + (63 - (jb >> 1)) * 80 - 80;
    bf16x8 a[4], b1[4], b0[4];
#define CV_RD(k, o) do { a[k] = ldsrd128(aa + (k) * 256); b1[k] = ldsrd128(bb + 2560 + (o)); if (TWO) b0[k] = ldsrd128(bb + (o)); } while (0)
    bb -= 2560;
    CV_RD(0, 112); CV_RD(1, 80); CV_RD(2, 32); CV_RD(3, 0);
#pragma unroll 1
    for (int j = jb; j < je; j += 4) {
        aa += 1024; bb -= 160;
        SBAR();
#define CV_ST(k, o) do { d1 = MFMA32(a[k], b1[k], d1); if (TWO) d0 = MFMA32(a[k], b0[k], d0); SBAR(); CV_RD(k, o); SBAR(); } while (0)
        CV_ST(0, 112); CV_ST(1, 80); CV_ST(2, 32); CV_ST(3, 0);
    }
#undef CV_ST
#undef CV_RD
}
__device__ __forceinline__ void conv_main(unsigned base, int w, int lane, f32x16& d0, f32x16& d1, bool poll, rsrc_t rs, unsigned epoch) {
    const int n = lane & 31, hh = lane >> 5, wg = w & 3;
    const unsigned aaddr = base + (n - 8 * hh + 9) * 16, baddr = base + CV_U + n * 80 + hh * 16;
    d0 = f32x16{}; d1 = f32x16{};
    const int jb = wg == 0 ? 0 : wg == 1 ? 24 : wg == 2 ? 48 : 84, je = wg == 0 ? 24 : wg == 1 ? 48 : wg == 2 ? 84 : 104;
    if (wg < 3) conv_range<true>(aaddr, baddr, jb, je < 64 ? je : 64, d0, d1);
#pragma unroll 1
    for (int part = 0; part < 2; ++part) {
        const int pb = part ? 104 : (jb > 64 ? jb : 64), pe = part ? 128 : je;
        if (wg >= 2 + part) conv_range<false>(aaddr, baddr, pb, pe, d0, d1);
        if (part == 0 && poll && w == 3) { poll_flags(rs, FL_QKV, 224, epoch, lane); asm volatile("buffer_inv sc1" ::: "memory"); }
    }
}
__device__ __forceinline__ void conv_partials(unsigned base, int wg, int lane, const f32x16& d0, const f32x16& d1) {
    const int n = lane & 31, hh = lane >> 5;
    const unsigned p = base + wg * 9216 + (36 * n + 4 * hh) * 4;
#pragma unroll
    for (int g = 0; g < 4; ++g) {
        if (wg < 3) *(LAS f32x4*)(uintptr_t)(p + g * 32) = (f32x4){d0[4 * g], d0[4 * g + 1], d0[4 * g + 2], d0[4 * g + 3]};
        *(LAS f32x4*)(uintptr_t)(p + 4608 + g * 32) = (f32x4){d1[4 * g], d1[4 * g + 1], d1[4 * g + 2], d1[4 * g + 3]};
    }
}
__device__ __forceinline__ void conv_finish(unsigned base, rsrc_t rs, const u16* __restrict__ ssm_D, int c, const u32x4& uu) {
    const int i = threadIdx.x & 255;
    const unsigned p = base + (8 * i + 4 * (i >> 2)) * 4;
    f32x4 lo = *(LAS const f32x4*)(uintptr_t)p, hi = *(LAS const f32x4*)(uintptr_t)(p + 16);
#pragma unroll
    for (int k = 1; k < 4; ++k)
        if (k < 3 || i >= 128) { lo += *(LAS const f32x4*)(uintptr_t)(p + k * 9216); hi += *(LAS const f32x4*)(uintptr_t)(p + k * 9216 + 16); }
    const float dd = bflo(ssm_D[c]);
    const float y[8] = {lo[0], lo[1], lo[2], lo[3], hi[0], hi[1], hi[2], hi[3]};
    unsigned o[4];
#pragma unroll
    for (int j = 0; j < 4; ++j) {
        const float u0 = bfhi(uu[3 - j]), u1 = bflo(uu[3 - j]);
        o[j] = cvtpk(rbf(y[2 * j]) + rbf(u0 * dd), rbf(y[2 * j + 1]) + rbf(u1 * dd));
    }
    st16(rs, (unsigned)WS_ZT + (unsigned)c * (S * 2) + i * 16, (u32x4){o[0], o[1], o[2], o[3]});
}
__device__ __forceinline__ unsigned attn_tile_off(int t) { return t < 16 ? 0u : 2u * CH_B; }
__device__ __forceinline__ void attn_slow(unsigned base, int lane, bf16x8 qf, s16x4 q2, f32x16& o, float& mrow) {
    const int m_ = lane & 31, h = lane >> 5, vrow = (m_ < 19) ? m_ : ((m_ == 22) ? 18 : 0);
    float m = -3.0e38f;
    o = f32x16{};
    for (int t = 0; t < 32; ++t) {
        const unsigned cb = base + attn_tile_off(t), tl = t & 15;
        const bf16x8 kf = ldsrd128(cb + tl * 1024 + lane * 16);
        const unsigned k2 = *(LAS const unsigned*)(uintptr_t)(cb + CH_K2 + tl * 128 + m_ * 4);
        f32x16 s = {};
        s = MFMA32(kf, qf, s);
        s = __builtin_amdgcn_mfma_f32_32x32x8bf16_1k(__builtin_bit_cast(s16x4, (u32x2){k2, 0u}), q2, s, 0, 0, 0);
        float tm = s[0];
#pragma unroll
        for (int r = 1; r < 16; ++r) tm = fmaxf(tm, s[r]);
        tm = fmaxf(tm, __shfl_xor(tm, 32));
        const float mn = fmaxf(m, tm);
        const float f = __builtin_amdgcn_exp2f(m - mn);
        m = mn;
#pragma unroll
        for (int r = 0; r < 16; ++r) { o[r] *= f; s[r] = __builtin_amdgcn_exp2f(s[r] - mn); }
        const f32x8 lo = __builtin_shufflevector(s, s, 0, 1, 2, 3, 4, 5, 6, 7), hi = __builtin_shufflevector(s, s, 8, 9, 10, 11, 12, 13, 14, 15);
        const unsigned va = cb + CH_VT + tl * 1216 + h * 304 + vrow * 16;
        o = __builtin_amdgcn_smfmac_f32_32x32x32_bf16(ldsrd128(va), __builtin_bit_cast(bf16x16, lo), o, (int)0xDDDDDDDD, 0, 0);
        o = __builtin_amdgcn_smfmac_f32_32x32x32_bf16(ldsrd128(va + 608), __builtin_bit_cast(bf16x16, hi), o, (int)0xDDDDDDDD, 0, 0);
    }
    mrow = m;
}
__device__ __forceinline__ bool attn_range_ok(const f32x16& o) {
    const float l = o[10];
    bool ok = (l >= 5.9604645e-8f) && (l <= 1.8446744e19f);
#pragma unroll
    for (int r = 0; r < 10; ++r) ok = ok && (fabsf(o[r]) <= 3.0e38f);
    return __all(ok);
}
__device__ __forceinline__ void attn_dma(const char* ws, int head, int first, unsigned lds0, int w, int lane) {
    const char* src = ws + WS_KV + (size_t)head * HEAD_B + first * CH_B + lane * 16;
#pragma unroll
    for (int i = 0; i < 10; ++i) { int p = w + 8 * i; p = p > 73 ? 73 : p; glds16(src + p * 1024, lds0 + first * CH_B + p * 1024); }
}
struct AttnQ { bf16x8 fA, fB; s16x4 rA, rB; unsigned a, b; };
__device__ __forceinline__ void attn_loadq(AttnQ& Q, const char* ws, int head, int qtA, int lane) {
    const char* qp = ws + WS_Q + (size_t)head * QH_B + (size_t)qtA * QT_B;
    asm volatile("global_load_dwordx4 %0, %1, off sc1" : "=v"(Q.fA) : "v"(qp + lane * 16) : "memory");
    asm volatile("global_load_dwordx4 %0, %1, off sc1" : "=v"(Q.fB) : "v"(qp + QT_B + lane * 16) : "memory");
    asm volatile("global_load_dword %0, %1, off sc1" : "=v"(Q.a) : "v"(qp + 1024 + (lane & 31) * 4) : "memory");
    asm volatile("global_load_dword %0, %1, off sc1" : "=v"(Q.b) : "v"(qp + QT_B + 1024 + (lane & 31) * 4) : "memory");
}
#define ATTN_QWAITBAR(Q, N) asm volatile("s_waitcnt vmcnt(" #N ") lgkmcnt(0)\n\ts_barrier" : "+v"(Q.fA), "+v"(Q.fB), "+v"(Q.a), "+v"(Q.b) :: "memory")
__device__ __forceinline__ void attn_fixq(AttnQ& Q, int lane) {
    Q.rA = __builtin_bit_cast(s16x4, (u32x2){lane < 32 ? Q.a : 0u, 0u}); Q.rB = __builtin_bit_cast(s16x4, (u32x2){lane < 32 ? Q.b : 0u, 0u});
}

__device__ __forceinline__ void attn_loop(AttnQ& Q, const char* ws, int head, unsigned lds0, int w, int lane, f32x16& oA, f32x16& oB) {
    const int kpart = w >> 2, m = lane & 31, h = lane >> 5;
    const int vrow = (m < 19) ? m : ((m == 22) ? 18 : 0);
    unsigned ka = lds0 + kpart * CH_B + lane * 16, k2a = lds0 + kpart * CH_B + CH_K2 + m * 4, va = lds0 + kpart * CH_B + CH_VT + h * 304 + vrow * 16;
    unsigned idx = 0xDDDDDDDDu;
    int cnt = 16;
    const unsigned long long vmask = (((1ull << 19) - 1) | (1ull << 22)) * 0x100000001ull;
    oA = f32x16{}; oB = f32x16{};
    const char* src2 = ws + WS_KV + (size_t)head * HEAD_B + 2 * CH_B;
    unsigned lane16 = lane * 16, ldsb = lds0 + 2 * CH_B, pnext = w;
#define EXP4(d0, d1, d2, d3, s0, s1, s2, s3) \
        "v_exp_f32 v" #d0 ", v" #s0 "\n v_exp_f32 v" #d1 ", v" #s1 "\n v_exp_f32 v" #d2 ", v" #s2 "\n v_exp_f32 v" #d3 ", v" #s3 "\n"
#define VRD(x) "s_mov_b64 exec, %12\n" x "s_mov_b64 exec, -1\n"
#define INLOOP_DMA "s_cmp_lt_u32 %10, 12\n s_cbranch_scc1 3f\n" DMA2 DMA2 "3:\n"
#define MIDBAR "s_waitcnt vmcnt(0)\n s_barrier\n"
#define DMA2 "s_min_u32 s44, %11, 73\n s_lshl_b32 s44, s44, 10\n s_add_u32 m0, %14, s44\n v_add_u32 v126, s44, %13\n s_add_u32 %11, %11, 8\n global_load_lds_dwordx4 v126, %15\n"
#define KEYTILE(PK, OK, PK2, OK2, OVH, PV, OVL, PL, PH) \
        "v_mfma_f32_32x32x16_bf16 v[80:95], v[112:115], %3, 0\n" \
        PK "ds_read_b128 v[112:115], %7 offset:" #OK "\n" \
        PL EXP4(96, 97, 98, 99, 64, 65, 66, 67) PH \
        "v_mfma_f32_32x32x8_bf16 v[80:95], v[116:117], %5, v[80:95]\n" \
        PK2 "ds_read_b32 v116, %8 offset:" #OK2 "\n" \
        EXP4(100, 101, 102, 103, 68, 69, 70, 71) \
        "v_smfmac_f32_32x32x32_bf16 %1, v[122:125], v[104:111], %6\n" \
        VRD("ds_read_b128 v[122:125], %9 offset:" #OVH "\n") \
        EXP4(104, 105, 106, 107, 72, 73, 74, 75) \
        "s_waitcnt lgkmcnt(3)\n" \
        "v_smfmac_f32_32x32x32_bf16 %0, v[118:121], v[96:103], %6\n" \
        EXP4(108, 109, 110, 111, 76, 77, 78, 79) \
        "s_waitcnt lgkmcnt(2)\n" \
        "v_mfma_f32_32x32x16_bf16 v[64:79], v[112:115], %2, 0\n" \
        PL EXP4(96, 97, 98, 99, 80, 81, 82, 83) PH \
        "s_waitcnt lgkmcnt(1)\n" \
        "v_mfma_f32_32x32x8_bf16 v[64:79], v[116:117], %4, v[64:79]\n" \
        EXP4(100, 101, 102, 103, 84, 85, 86, 87) \
        "s_waitcnt lgkmcnt(0)\n" \
        "v_smfmac_f32_32x32x32_bf16 %0, v[122:125], v[104:111], %6\n" \
        EXP4(104, 105, 106, 107, 88, 89, 90, 91) \
        "v_smfmac_f32_32x32x32_bf16 %1, v[118:121], v[96:103], %6\n" \
        PV VRD("ds_read_b128 v[118:121], %9 offset:" #OVL "\n") \
        EXP4(108, 109, 110, 111, 92, 93, 94, 95)
#define LOOPASM(PL, PH, PRE) \
    asm volatile( \
        PRE \
        "ds_read_b128 v[112:115], %7\n" \
        "ds_read_b32 v116, %8\n" \
        "v_mov_b32 v117, 0\n" \
        "v_mov_b32 v104, 0\n v_mov_b32 v105, 0\n v_mov_b32 v106, 0\n v_mov_b32 v107, 0\n" \
        "v_mov_b32 v108, 0\n v_mov_b32 v109, 0\n v_mov_b32 v110, 0\n v_mov_b32 v111, 0\n" \
        "v_mov_b32 v122, 0\n v_mov_b32 v123, 0\n v_mov_b32 v124, 0\n v_mov_b32 v125, 0\n" \
        "v_mov_b32 v118, 0\n v_mov_b32 v119, 0\n v_mov_b32 v120, 0\n v_mov_b32 v121, 0\n" \
        "s_waitcnt lgkmcnt(0)\n" \
        "v_mfma_f32_32x32x16_bf16 v[64:79], v[112:115], %2, 0\n" \
        "v_mfma_f32_32x32x8_bf16 v[64:79], v[116:117], %4, v[64:79]\n" \
        VRD("ds_read_b128 v[118:121], %9\n") \
        "s_nop 7\n s_nop 7\n" \
        "s_mov_b32 s43, m0\n" \
        "1:\n" \
        INLOOP_DMA \
        "s_movk_i32 s40, 2048\n s_movk_i32 s41, 256\n s_movk_i32 s42, 2432\n" \
        "s_cmp_lg_u32 %10, 9\n s_cbranch_scc1 2f\n" \
        MIDBAR \
        "s_mov_b32 s40, 61440\n s_mov_b32 s41, 73984\n s_mov_b32 s42, 58752\n" \
        "2:\n" \
        KEYTILE("", 1024, "", 128, 608, "", 1216, PL, PH) \
        KEYTILE("v_add_u32 %7, s40, %7\n", 0, "v_add_u32 %8, s41, %8\n", 0, 1824, "v_add_u32 %9, s42, %9\n", 0, PL, PH) \
        "s_sub_u32 %10, %10, 1\n s_cmp_lg_u32 %10, 0\n s_cbranch_scc1 1b\n" \
        "s_waitcnt lgkmcnt(0)\n" \
        "v_smfmac_f32_32x32x32_bf16 %1, v[122:125], v[104:111], %6\n" \
        "s_mov_b32 m0, s43\n s_setprio 0\n s_nop 7\n s_nop 7\n" \
        : "+v"(oA), "+v"(oB), "+v"(Q.fA), "+v"(Q.fB), "+v"(Q.rA), "+v"(Q.rB), "+v"(idx), "+v"(ka), "+v"(k2a), "+v"(va), "+s"(cnt), "+s"(pnext) \
        : "s"(vmask), "v"(lane16), "s"(ldsb), "s"(src2) \
        : "v64", "v65", "v66", "v67", "v68", "v69", "v70", "v71", "v72", "v73", "v74", "v75", "v76", "v77", "v78", "v79", \
          "v80", "v81", "v82", "v83", "v84", "v85", "v86", "v87", "v88", "v89", "v90", "v91", "v92", "v93", "v94", "v95", \
          "v96", "v97", "v98", "v99", "v100", "v101", "v102", "v103", "v104", "v105", "v106", "v107", "v108", "v109", "v110", "v111", \
          "v112", "v113", "v114", "v115", "v116", "v117", "v118", "v119", "v120", "v121", "v122", "v123", "v124", "v125", \
          "v126", "s40", "s41", "s42", "s43", "s44", "scc", "memory")
    if (w < 4) { LOOPASM("s_setprio 0\n", "s_setprio 2\n", "s_setprio 2\n"); } else { LOOPASM("", "", "s_setprio 1\n"); }
#undef LOOPASM
#undef KEYTILE
#undef DMA2
#undef VRD
#undef EXP4
}
__device__ __forceinline__ void attn_finish(AttnQ& Q, f32x16& oA, f32x16& oB, rsrc_t rs, int head, int qtA, unsigned lds0, int w, int lane) {
    const int kpart = w >> 2, m = lane & 31, h = lane >> 5;
    float mA = 0.f, mB = 0.f;
    const bool okA = attn_range_ok(oA), okB = attn_range_ok(oB);
    if (!(okA && okB)) {
#pragma unroll 1
        for (int i = 0; i < 2; ++i) {
            if (i ? okB : okA) continue;
            f32x16 o; float mr;
            attn_slow(lds0 + kpart * CH_B, lane, i ? Q.fB : Q.fA, i ? Q.rB : Q.rA, o, mr);
            if (i) { oB = o; mB = mr; } else { oA = o; mA = mr; }
        }
    }
    LBAR();
    {
        LAS float* xch = (LAS float*)(uintptr_t)(lds0 + w * 3072);
        const f32x16& give = kpart ? oA : oB;
#pragma unroll
        for (int r = 0; r < 11; ++r) xch[r * 64 + lane] = give[r];
        xch[11 * 64 + lane] = kpart ? mA : mB;
    }
    LBAR();
    {
        LAS const float* xin = (LAS const float*)(uintptr_t)(lds0 + (w ^ 4) * 3072);
        const f32x16& keep = kpart ? oB : oA;
        const float m0 = kpart ? mB : mA, m1 = xin[11 * 64 + lane];
        const float mm = fmaxf(m0, m1), f0 = __builtin_amdgcn_exp2f(m0 - mm), f1 = __builtin_amdgcn_exp2f(m1 - mm);
        float v[11];
#pragma unroll
        for (int r = 0; r < 11; ++r) v[r] = keep[r] * f0 + xin[r * 64 + lane] * f1;
        const float inv = 1.0f / v[10];
        const int tb = qtA + kpart;
        const u32x4 o8 = {cvtpk(v[0] * inv, v[1] * inv), cvtpk(v[2] * inv, v[3] * inv), cvtpk(v[4] * inv, v[5] * inv), cvtpk(v[6] * inv, v[7] * inv)};
        st16(rs, (unsigned)WS_OM + ((2 * tb + (m >> 4)) * 16 + (head >> 1)) * 1024 + ((2 * (head & 1) + h) * 16 + (m & 15)) * 16, o8);
        const unsigned rr = cvtpk(v[8] * inv, v[9] * inv);
        const unsigned r0 = __shfl(rr, (lane & 7) * 4), r1 = __shfl(rr, (lane & 7) * 4 + 1), r2 = __shfl(rr, (lane & 7) * 4 + 2), r3 = __shfl(rr, (lane & 7) * 4 + 3);
        if (lane < 8) st16(rs, (unsigned)WS_OR + (tb * 32 + head) * 128 + lane * 16, (u32x4){r0, r1, r2, r3});
    }
}

constexpr int P4_SLAB = 24576, P4_ZT = P4_SLAB + 576 * 144, P4_RED = P4_ZT + 72 * 144;
__device__ __forceinline__ void proj_dma(const u16* __restrict__ wo, int cg, unsigned lds0, int w, int lane) {
#pragma unroll
    for (int i = 0; i < 11; ++i) {
        int p = w + 8 * i; p = p > 80 ? 80 : p;
        const int ck = p * 64 + lane, row = ck / 9, c9 = ck - row * 9;
        glds16(wo + (size_t)row * D + cg * 72 + c9 * 8, lds0 + P4_SLAB + p * 1024);
    }
}
struct GateRegs { u32x4 z0, z1, q0, q1; };
__device__ __forceinline__ void gate_issue(GateRegs& R, rsrc_t rs, const u16* __restrict__ bq, int tb, int cg, int w, int lane) {
    const int tid = threadIdx.x;
    R.z0 = ld16(rs, (unsigned)WS_ZT + (unsigned)(cg * 72 + (tid >> 3)) * (S * 2) + (tb * 64 + (tid & 7) * 8) * 2);
    R.z1 = R.z0;
    if (tid < 64) R.z1 = ld16(rs, (unsigned)WS_ZT + (unsigned)(cg * 72 + 64 + (tid >> 3)) * (S * 2) + (tb * 64 + (tid & 7) * 8) * 2);
    const int G = lane >> 4;
    { const int u = 4 * w + G, tg = u / 9, cb = u - tg * 9; R.q0 = *(const u32x4*)(bq + (size_t)(tb * 64 + tg * 16 + (lane & 15)) * D + cg * 72 + cb * 8); }
    R.q1 = R.q0;
    if (w == 0) { const int u = 32 + G, tg = u / 9, cb = u - tg * 9; R.q1 = *(const u32x4*)(bq + (size_t)(tb * 64 + tg * 16 + (lane & 15)) * D + cg * 72 + cb * 8); }
}
__device__ __forceinline__ void gate_stage(const GateRegs& R, unsigned lds0) {
    const int tid = threadIdx.x;
    ldswr4(lds0 + P4_ZT + (tid >> 3) * 144 + (tid & 7) * 16, R.z0);
    if (tid < 64) ldswr4(lds0 + P4_ZT + (64 + (tid >> 3)) * 144 + (tid & 7) * 16, R.z1);
}
__device__ __forceinline__ void gate_unit(unsigned lds0, u16* __restrict__ out, const u32x4& qq, int u, int tb, int cg, int lane) {
    const int tg = u / 9, cb = u - tg * 9, q = (lane & 15) >> 2, p = lane & 3;
    const unsigned a = lds0 + P4_ZT + (8 * cb + q) * 144 + (16 * tg + 4 * p) * 2;
    const u32x2 zl = __builtin_bit_cast(u32x2, trread(a)), zh = __builtin_bit_cast(u32x2, trread(a + 4 * 144));
    const u32x4 r = {cvtpk(bflo(qq[0]) * bflo(zl[0]), bfhi(qq[0]) * bfhi(zl[0])), cvtpk(bflo(qq[1]) * bflo(zl[1]), bfhi(qq[1]) * bfhi(zl[1])),
                     cvtpk(bflo(qq[2]) * bflo(zh[0]), bfhi(qq[2]) * bfhi(zh[0])), cvtpk(bflo(qq[3]) * bflo(zh[1]), bfhi(qq[3]) * bfhi(zh[1]))};
    *(u32x4*)(out + (size_t)(tb * 64 + tg * 16 + (lane & 15)) * OUTW + D + cg * 72 + cb * 8) = r;
}
__device__ __forceinline__ void gate_finish(const GateRegs& R, unsigned lds0, u16* out, int tb, int cg, int w, int lane) {
    gate_unit(lds0, out, R.q0, 4 * w + (lane >> 4), tb, cg, lane);
    if (w == 0) gate_unit(lds0, out, R.q1, 32 + (lane >> 4), tb, cg, lane);
}
struct ProjW { bf16x8 f[9][5]; };
__device__ __forceinline__ void proj_readw(ProjW& W, unsigned lds0, int w, int lane) {
    const int kh = w >> 2, kq = lane >> 4, q = (lane & 15) >> 2, p = lane & 3;
#pragma unroll
    for (int s = 0; s < 9; ++s) {
        const int st = kh * 9 + s;
        int r0, r1;
        if (st < 16) { const int hd = 2 * st + (kq >> 1), hh = kq & 1; r0 = 18 * hd + 4 * hh + q; r1 = r0 + 8; }
        else { const int hd = 16 * (st - 16) + 4 * kq + (q >> 1); r0 = 18 * hd + 16 + (q & 1); r1 = r0 + 36; }
#pragma unroll
        for (int c = 0; c < 5; ++c) {
            const int cc = (c == 4 && p >= 2) ? 64 + (p & 1) * 4 : 16 * c + 4 * p;
            W.f[s][c] = cat8(trread(lds0 + P4_SLAB + r0 * 144 + cc * 2), trread(lds0 + P4_SLAB + r1 * 144 + cc * 2));
        }
    }
}
__device__ __forceinline__ void proj_tile(const ProjW& W, rsrc_t rs, u16* __restrict__ out, int tb64, int cg, unsigned lds0, int w, int lane) {
    const int tt = w & 3, kh = w >> 2, t16 = tb64 * 4 + tt, kq = lane >> 4;
    bf16x8 af[9];
#pragma unroll
    for (int s = 0; s < 9; ++s) {
        const int st = kh * 9 + s;
        if (st < 16) af[s] = __builtin_bit_cast(bf16x8, ld16(rs, (unsigned)WS_OM + (t16 * 16 + st) * 1024 + lane * 16));
        else {
            const unsigned ro = (unsigned)WS_OR + ((t16 >> 1) * 32 + 16 * (st - 16) + 4 * (lane >> 4)) * 128 + ((t16 & 1) * 16 + (lane & 15)) * 4;
            af[s] = __builtin_bit_cast(bf16x8, (u32x4){ld4(rs, ro), ld4(rs, ro + 128), ld4(rs, ro + 256), ld4(rs, ro + 384)});
        }
    }
    f32x4 acc[5] = {};
#pragma unroll
    for (int s = 0; s < 9; ++s)
#pragma unroll
        for (int c = 0; c < 5; ++c) acc[c] = MFMA16(W.f[s][c], af[s], acc[c]);
    LAS f32x4* red = (LAS f32x4*)(uintptr_t)(lds0 + P4_RED + tt * 5120);
    if (kh == 1) {
#pragma unroll
        for (int c = 0; c < 5; ++c) red[c * 64 + lane] = acc[c];
    }
    LBAR();
    if (kh == 0) {
        u16* op = out + (size_t)(t16 * 16 + (lane & 15)) * OUTW + cg * 72 + 4 * kq;
#pragma unroll
        for (int c = 0; c < 5; ++c) {
            const f32x4 v = acc[c] + red[c * 64 + lane];
            if (c < 4 || kq < 2) *(u32x2*)(op + 16 * c) = (u32x2){cvtpk(v[0], v[1]), cvtpk(v[2], v[3])};
        }
    }
}

__device__ __forceinline__ void conv_phase(const u16* ssm_k, const u16* ssm_D, char* ws, rsrc_t rs, int pair, bool pubqkv, bool last,
                                           AttnQ& Q, int head, int qtA, unsigned epoch, unsigned lds0, int w, int lane STAMP_ARG) {
    const int grp = w >> 2, c = 2 * pair + grp;
    const unsigned base = lds0 + CV_BASE + grp * CV_B;
    ConvRegs R;
    conv_issue_h(R, ssm_k, c);
    if (w == 0) poll_flags(rs, FL_KV + (pair >> 5) * 128, 32, epoch, lane);
    LBAR();
    conv_issue_u(R, rs, c);
    conv_build(R, base);
    conv_build_u(R, base);
    WAITBAR(0);
    if (pubqkv) publish(rs, FL_QKV + blockIdx.x * 4, epoch);
    STAMP(1, 3);
    f32x16 d0, d1;
    conv_main(base, w, lane, d0, d1, last, rs, epoch);
    STAMP(1, 4);
    WAITBAR(0);
    if (last) { attn_loadq(Q, ws, head, qtA, lane); attn_dma(ws, head, 0, lds0, w, lane); }
    conv_partials(base, w & 3, lane, d0, d1);
    LBAR();
    conv_finish(base, rs, ssm_D, c, R.u);
}

__global__ __launch_bounds__(512, 2) void fused(const u16* __restrict__ x, const u16* __restrict__ wqkv, const u16* __restrict__ wo,
                                                const u16* __restrict__ bq, const u16* __restrict__ bk, const u16* __restrict__ bv,
                                                const u16* __restrict__ shift_k, const u16* __restrict__ ssm_k, const u16* __restrict__ ssm_D,
                                                char* __restrict__ ws, u16* __restrict__ out) {
    extern __shared__ __attribute__((aligned(16))) char lds[];
    const unsigned lds0 = (unsigned)(uintptr_t)lds;
    const int lane = threadIdx.x & 63, w = __builtin_amdgcn_readfirstlane(threadIdx.x >> 6), b = blockIdx.x;
    const rsrc_t rs = ws_rsrc(ws);
    STAMP_DECL;
    STAMP(0, 0);
    const unsigned epoch = *(const __attribute__((address_space(4))) unsigned*)(uintptr_t)ws + 1u;
    phase1(x, wqkv, bk, bv, shift_k, ws, rs, lds0, w, lane, epoch STAMP_PASS);
    STAMP(0, 2);
    const int head = (b & 7) * 4 + ((b >> 3) & 3), qb = b >> 5, qtA = qb * 8 + (w & 3) * 2;
    AttnQ Q;
    const int rounds = b < 224 ? 1 : 2, pair0 = b < 224 ? b : 224 + 2 * (b - 224);
#pragma unroll 1
    for (int rd = 0; rd < rounds; ++rd)
        conv_phase(ssm_k, ssm_D, ws, rs, pair0 + rd, b < 224, rd + 1 == rounds, Q, head, qtA, epoch, lds0, w, lane STAMP_PASS);
    ATTN_QWAITBAR(Q, 0);
    publish(rs, FL_Z + b * 4, epoch);
    attn_fixq(Q, lane);
    STAMP(1, 5);
    f32x16 oA, oB;
    attn_loop(Q, ws, head, lds0, w, lane, oA, oB);
    STAMP(1, 6);
    attn_finish(Q, oA, oB, rs, head, qtA, lds0, w, lane);
    const int i5 = b & 31, ptb = (b >> 5) * 4 + ((i5 & 7) >> 1), pcg = (i5 & 1) * 4 + (i5 >> 3);
    proj_dma(wo, pcg, lds0, w, lane);
    if (w == 1) poll_flags(rs, FL_Z, 256, epoch, lane);
    WAITBAR(11);
    publish(rs, FL_O + b * 4, epoch);
    if (b == 0 && threadIdx.x == 0) *(unsigned*)ws = epoch;
    GateRegs G;
    gate_issue(G, rs, bq, b >> 3, b & 7, w, lane);
    gate_stage(G, lds0);
    WAITBAR(0);
    gate_finish(G, lds0, out, b >> 3, b & 7, w, lane);
    ProjW W;
    proj_readw(W, lds0, w, lane);
    if (w == 0) poll_flags(rs, FL_O + (b >> 5) * 128, 32, epoch, lane);
    LBAR();
    proj_tile(W, rs, out, ptb, pcg, lds0, w, lane);
    STAMP(1, 7);
    STAMP_FLUSH(0);
}

extern "C" void kernel_launch(void* const* d_in, const int* in_sizes, int n_in, void* d_out, int out_size, void* d_ws, size_t ws_size,
                              hipStream_t stream) {
    (void)hipFuncSetAttribute((const void*)fused, hipFuncAttributeMaxDynamicSharedMemorySize, LDS_BYTES);
    fused<<<256, 512, LDS_BYTES, stream>>>((const u16*)d_in[0], (const u16*)d_in[1], (const u16*)d_in[2], (const u16*)d_in[3], (const u16*)d_in[4],
                                           (const u16*)d_in[5], (const u16*)d_in[6], (const u16*)d_in[7], (const u16*)d_in[8], (char*)d_ws, (u16*)d_out);
}
```

```cpp
#include <hip/hip_runtime.h>
#include <hip/hip_bf16.h>
#include <stdint.h>

#define LAS __attribute__((address_space(3)))
typedef __attribute__((ext_vector_type(8))) __bf16 bf16x8;
typedef __attribute__((ext_vector_type(16))) __bf16 bf16x16;
typedef __attribute__((ext_vector_type(4))) short s16x4;
typedef __attribute__((ext_vector_type(8))) short s16x8;
typedef __attribute__((ext_vector_type(16))) float f32x16;
typedef __attribute__((ext_vector_type(8))) float f32x8;
typedef __attribute__((ext_vector_type(4))) float f32x4;
typedef __attribute__((ext_vector_type(4))) unsigned u32x4;
typedef __attribute__((ext_vector_type(2))) unsigned u32x2;
typedef unsigned short u16;

constexpr int S = 2048, D = 576, NH = 32;
constexpr int OUTW = 2 * D;
constexpr int CH_K2 = 16384, CH_VT = 18432, CH_B = 36864, HEAD_B = 4 * CH_B;
constexpr int QT_B = 1152, QH_B = 64 * QT_B;
constexpr size_t WS_FLAG = 0;
constexpr unsigned FL_GEN = 0, FL_KV = 1024, FL_Z = 3584, FL_O = 8192, FL_QKV = FL_O + 256 * 128;
constexpr size_t WS_KV = FL_QKV + 256 * 1024;
constexpr size_t WS_Q = WS_KV + (size_t)NH * HEAD_B;
constexpr size_t WS_OM = WS_Q + (size_t)NH * QH_B;
constexpr size_t WS_OR = WS_OM + (size_t)128 * 16 * 1024;
constexpr size_t WS_KVT = WS_OR + (size_t)64 * 32 * 128;
constexpr size_t WS_ZT = WS_KVT + (size_t)S * D * 2;
constexpr size_t WS_END = WS_ZT + (size_t)S * D * 2;
constexpr float QSCALE = 0.23570226039551584f * 1.4426950408889634f;
constexpr unsigned OOB = 0xFFFFFF00u;
constexpr int LDS_CNT = 158720, LDS_BYTES = LDS_CNT + 64;

#define STAMP_DECL
#define STAMP(kid, pt)
#define STAMP_FLUSH(kid)
#define STAMP_ARG
#define STAMP_PASS
#define GST(pt)
#define SBAR() __builtin_amdgcn_sched_barrier(0)
#define MFMA32(a, b, c) __builtin_amdgcn_mfma_f32_32x32x16_bf16(a, b, c, 0, 0, 0)
#define MFMA16(a, b, c) __builtin_amdgcn_mfma_f32_16x16x32_bf16(a, b, c, 0, 0, 0)
#define WAITBAR(V) asm volatile("s_waitcnt vmcnt(" #V ") lgkmcnt(0)\n\ts_barrier" ::: "memory")
#define LBAR() asm volatile("s_waitcnt lgkmcnt(0)\n\ts_barrier" ::: "memory")

__device__ __forceinline__ void glds16(const void* g, unsigned lds_base) {
    asm volatile("s_mov_b32 m0, %1\n\ts_nop 0\n\tglobal_load_lds_dwordx4 %0, off" :: "v"(g), "s"(lds_base) : "memory");
}
__device__ __forceinline__ unsigned cvtpk(float lo, float hi) {
    typedef __attribute__((ext_vector_type(2))) float f2; typedef __attribute__((ext_vector_type(2))) __bf16 b2;
    f2 v = {lo, hi};
    return __builtin_bit_cast(unsigned, __builtin_convertvector(v, b2));
}
__device__ __forceinline__ float bflo(unsigned u) { return __uint_as_float(u << 16); }
__device__ __forceinline__ float bfhi(unsigned u) { return __uint_as_float(u & 0xffff0000u); }
__device__ __forceinline__ float rbf(float x) { return bflo(cvtpk(x, 0.f)); }
__device__ __forceinline__ s16x4 trread(unsigned a) {
    return __builtin_bit_cast(s16x4, __builtin_amdgcn_ds_read_tr16_b64_v4i16((LAS s16x4*)(uintptr_t)a));
}
__device__ __forceinline__ bf16x8 cat8(s16x4 a, s16x4 b) { return __builtin_bit_cast(bf16x8, (s16x8)__builtin_shufflevector(a, b, 0, 1, 2, 3, 4, 5, 6, 7)); }
__device__ __forceinline__ bf16x8 ldsrd128(unsigned a) { return *(LAS const bf16x8*)(uintptr_t)a; }
__device__ __forceinline__ u32x4 ldsrdu4(unsigned a) { return *(LAS const u32x4*)(uintptr_t)a; }
__device__ __forceinline__ u32x2 ldsrdu2(unsigned a) { return *(LAS const u32x2*)(uintptr_t)a; }
__device__ __forceinline__ void ldswr4(unsigned a, u32x4 v) { *(LAS u32x4*)(uintptr_t)a = v; }
__device__ __forceinline__ void ldswr2(unsigned a, u32x2 v) { *(LAS u32x2*)(uintptr_t)a = v; }
__device__ __forceinline__ void ldswr1(unsigned a, unsigned v) { *(LAS unsigned*)(uintptr_t)a = v; }

__device__ __forceinline__ unsigned l2touch(const void* p) { unsigned r; asm volatile("global_load_dword %0, %1, off" : "=v"(r) : "v"(p) : "memory"); return r; }
#define KEEP(x) asm volatile("" :: "v"(x))

typedef __amdgpu_buffer_rsrc_t rsrc_t;
__device__ __forceinline__ rsrc_t ws_rsrc(char* ws) { return __builtin_amdgcn_make_buffer_rsrc(ws, 0, (int)WS_END, 0x00020000); }
__device__ __forceinline__ void st16(rsrc_t r, unsigned off, u32x4 v) { __builtin_amdgcn_raw_buffer_store_b128(v, r, off, 0, 16); }
__device__ __forceinline__ void st8(rsrc_t r, unsigned off, u32x2 v) { __builtin_amdgcn_raw_buffer_store_b64(v, r, off, 0, 16); }
__device__ __forceinline__ void st4(rsrc_t r, unsigned off, unsigned v) { __builtin_amdgcn_raw_buffer_store_b32(v, r, off, 0, 16); }
__device__ __forceinline__ u32x4 ld16(rsrc_t r, unsigned off) { return __builtin_amdgcn_raw_buffer_load_b128(r, off, 0, 16); }
__device__ __forceinline__ unsigned ld4(rsrc_t r, unsigned off) { return __builtin_amdgcn_raw_buffer_load_b32(r, off, 0, 16); }

__device__ __forceinline__ void publish(rsrc_t rs, unsigned off, unsigned epoch) { if (threadIdx.x == 0) st4(rs, off, epoch); }
__device__ __forceinline__ void publish_qkv(rsrc_t rs, int mb, int ng, unsigned epoch, int lane) {
    const int c0 = ng * 128 - (ng >= 9 ? 1152 : ng >= 5 ? 576 : 0), h0 = c0 / 18 , h1 = min((c0 + 127) / 18, 31);
#pragma unroll
    for (int k = 0; k < 2; ++k) {
        const int l = lane + 64 * k, hi = l >> 3, qb = l & 7;
        int head = h0 + hi; bool ok = head <= h1;
        if (ng == 4) { head = (28 + hi) & 31; ok = hi < 8; }
        const int consumer = (qb << 5) | ((head & 3) << 3) | (head >> 2);
        st4(rs, ok ? FL_QKV + consumer * 1024 + (ng * 16 + mb) * 4 : 0xFFFFFF00u, epoch);
    }
}
__device__ __forceinline__ void poll_at(rsrc_t rs, unsigned off, bool act, unsigned epoch) {
#pragma unroll 1
    for (int spin = 0; spin < (1 << 18); ++spin) {
        bool ok = true;
        if (act) { const u32x4 v = ld16(rs, off); ok = v[0] == epoch && v[1] == epoch && v[2] == epoch && v[3] == epoch; }
        if (__all(ok)) break;
        __builtin_amdgcn_s_sleep(2);
        asm volatile("" ::: "memory");
    }
    asm volatile("" ::: "memory");
}
__device__ __forceinline__ void poll_flags(rsrc_t rs, unsigned off, int nwords, unsigned epoch, int lane) {
#pragma unroll 1
    for (int spin = 0; spin < (1 << 18); ++spin) {
        bool ok = true;
        if (lane * 4 < nwords) { const u32x4 v = ld16(rs, off + lane * 16); ok = v[0] == epoch && v[1] == epoch && v[2] == epoch && v[3] == epoch; }
        if (__all(ok)) break;
        __builtin_amdgcn_s_sleep(4);
        asm volatile("" ::: "memory");
    }
    asm volatile("" ::: "memory");
}

constexpr int KVS_K = 0, KVS_W = 67 * 144, KVS_T = KVS_W + 512, KVS_B = KVS_T + 64 * 144;
struct KvRegs { u32x4 k, v, x; };
__device__ __forceinline__ u32x4 gload16(const void* p) {
    u32x4 r; asm volatile("global_load_dwordx4 %0, %1, off" : "=v"(r) : "v"(p) : "memory"); return r;
}
__device__ __forceinline__ void kv_issue(KvRegs& R, const u16* __restrict__ bk, const u16* __restrict__ bv, const u16* __restrict__ shift_k, int tb, int cg) {
    const int tid = threadIdx.x, t = tb * 64 + (tid >> 3), c = cg * 64 + (tid & 7) * 8;
    R.k = gload16(bk + (size_t)t * D + c);
    R.v = gload16(bv + (size_t)t * D + c);
    const u16* xp = bk + c;
    if (tid < 24) { const int th = tb * 64 - 3 + (tid >> 3); xp = bk + (size_t)(th < 0 ? 0 : th) * D + c; }
    else if (tid >= 64 && tid < 96) xp = shift_k + cg * 256 + (tid - 64) * 8;
    R.x = gload16(xp);
}
#define KV_WAIT(R, N) asm volatile("s_waitcnt vmcnt(" #N ")" : "+v"(R.k), "+v"(R.v), "+v"(R.x) :: "memory")
__device__ __forceinline__ void kv_stage(const KvRegs& R, int tb, unsigned sc) {
    const int tid = threadIdx.x;
    ldswr4(sc + KVS_K + ((tid >> 3) + 3) * 144 + (tid & 7) * 16, R.k);
    if (tid < 24) ldswr4(sc + KVS_K + (tid >> 3) * 144 + (tid & 7) * 16, (tb * 64 - 3 + (tid >> 3) < 0) ? (u32x4){0u, 0u, 0u, 0u} : R.x);
    else if (tid >= 64 && tid < 96) ldswr4(sc + KVS_W + (tid - 64) * 16, R.x);
}
__device__ __forceinline__ float dot2bf(unsigned a, unsigned b, float c) {
    typedef __attribute__((ext_vector_type(2))) __bf16 b2;
    return __builtin_amdgcn_fdot2_f32_bf16(__builtin_bit_cast(b2, a), __builtin_bit_cast(b2, b), c, false);
}
__device__ __forceinline__ unsigned kv_pair(unsigned k0, unsigned k1, unsigned k2, unsigned k3, unsigned vv, unsigned wa0, unsigned wa1, unsigned wb0, unsigned wb1) {
    const float sl = dot2bf(__builtin_amdgcn_perm(k2, k3, 0x05040100u), wa0, dot2bf(__builtin_amdgcn_perm(k0, k1, 0x05040100u), wa1, 0.f));
    const float sh = dot2bf(__builtin_amdgcn_perm(k2, k3, 0x07060302u), wb0, dot2bf(__builtin_amdgcn_perm(k0, k1, 0x07060302u), wb1, 0.f));
    const unsigned s = cvtpk(sl, sh);
    return cvtpk(bflo(s) * bflo(vv), bfhi(s) * bfhi(vv));
}
__device__ __forceinline__ void kv_compute(const KvRegs& R, unsigned sc) {
    const int tid = threadIdx.x, trow = tid >> 3, ch = tid & 7;
    const unsigned ka = sc + KVS_K + trow * 144 + ch * 16;
    const u32x4 k0 = ldsrdu4(ka), k1 = ldsrdu4(ka + 144), k2 = ldsrdu4(ka + 288);
    const unsigned wa = sc + KVS_W + ch * 64;
    const u32x4 w0 = ldsrdu4(wa), w1 = ldsrdu4(wa + 16), w2 = ldsrdu4(wa + 32), w3 = ldsrdu4(wa + 48);
    const u32x4 r = {kv_pair(k0[0], k1[0], k2[0], R.k[0], R.v[0], w0[0], w0[1], w0[2], w0[3]),
                     kv_pair(k0[1], k1[1], k2[1], R.k[1], R.v[1], w1[0], w1[1], w1[2], w1[3]),
                     kv_pair(k0[2], k1[2], k2[2], R.k[2], R.v[2], w2[0], w2[1], w2[2], w2[3]),
                     kv_pair(k0[3], k1[3], k2[3], R.k[3], R.v[3], w3[0], w3[1], w3[2], w3[3])};
    const int row = (trow & ~7) | (7 - (trow & 7));
    ldswr4(sc + KVS_T + row * 144 + ch * 16, r);
}
__device__ __forceinline__ void kv_store(rsrc_t rs, int tb, int cg, unsigned sc, int w, int lane) {
    const int G = lane >> 4, q = (lane & 15) >> 2, p = lane & 3, beta = (w >> 2) * 4 + G;
    const unsigned a = sc + KVS_T + (8 * beta + q) * 144 + (16 * (w & 3) + 4 * p) * 2;
    const s16x4 lo = trread(a), hi = trread(a + 4 * 144);
    st16(rs, (unsigned)WS_KVT + (unsigned)(cg * 64 + 16 * (w & 3) + (lane & 15)) * (S * 2) + (tb * 64 + 8 * beta) * 2,
         __builtin_bit_cast(u32x4, (s16x8)__builtin_shufflevector(lo, hi, 0, 1, 2, 3, 4, 5, 6, 7)));
}
__device__ __forceinline__ void kv_only(const u16* bk, const u16* bv, const u16* shift_k, rsrc_t rs, unsigned lds0, int w, int lane, unsigned epoch) {
    const int t = 224 + ((int)blockIdx.x - 224) * 2;
    KvRegs R0, R1;
    kv_issue(R0, bk, bv, shift_k, t & 31, t >> 5); kv_issue(R1, bk, bv, shift_k, (t + 1) & 31, (t + 1) >> 5);
    KV_WAIT(R0, 3); kv_stage(R0, t & 31, lds0);
    KV_WAIT(R1, 0); kv_stage(R1, (t + 1) & 31, lds0 + KVS_B);
    LBAR();
    kv_compute(R0, lds0); kv_compute(R1, lds0 + KVS_B);
    LBAR();
    kv_store(rs, t & 31, t >> 5, lds0, w, lane); kv_store(rs, (t + 1) & 31, (t + 1) >> 5, lds0 + KVS_B, w, lane);
    WAITBAR(0);
    publish(rs, FL_KV + t * 4, epoch);
    if (threadIdx.x == 0) st4(rs, FL_KV + (t + 1) * 4, epoch);
}

template <bool B> struct BoolC { static constexpr bool value = B; };
constexpr int G1_STAGE = 32768, G1_SC = 4 * G1_STAGE;

struct ConvRegs { u32x4 h0, h1; unsigned h2; u32x4 u; };
__device__ __forceinline__ void conv_issue(ConvRegs& R, const u16* __restrict__ ssm_k, rsrc_t rs, int c, int w);

__device__ __forceinline__ void qkv_tile(const bool ISV, const u16* __restrict__ x, const u16* __restrict__ wqkv, rsrc_t rs,
                                         int mb, int ng, unsigned lds0, int w, int lane,
                                         const u16* __restrict__ bk, const u16* __restrict__ bv, const u16* __restrict__ shift_k, int kvtb, int kvcg,
                                         unsigned epoch, ConvRegs& CR, const u16* __restrict__ ssm_k, int pair STAMP_ARG) {
    const int ctl = w & 3, tb0 = (w >> 2) * 2;
    const bool active = (ng * 4 + ctl) < 54;
    const u16* xsrc[2]; const u16* wsrc[2];
#pragma unroll
    for (int i = 0; i < 2; ++i) {
        const int p = w * 2 + i;
        const int xr = 8 * p + (lane >> 3), xc = (lane & 7) ^ ((xr >> 1) & 7);
        xsrc[i] = x + (size_t)(mb * 128 + xr) * D + xc * 8;
        const int wr = 4 * p + (lane >> 4), wc = (lane & 15) ^ (((wr & 3) << 2) | ((wr >> 2) & 3));
        int col = ng * 128 + wc * 8; col = col > 1720 ? 1720 : col;
        wsrc[i] = wqkv + (size_t)wr * (3 * D) + col;
    }
    auto issue = [&](int c) {
#pragma unroll
        for (int i = 0; i < 2; ++i) {
            glds16(xsrc[i] + c * 64, lds0 + (c & 3) * G1_STAGE + (w * 2 + i) * 1024);
            glds16(wsrc[i] + c * 64 * (3 * D), lds0 + (c & 3) * G1_STAGE + 16384 + (w * 2 + i) * 1024);
        }
    };
    const int r = lane & 31, h = lane >> 5;
    unsigned xa[4];
#pragma unroll
    for (int s = 0; s < 4; ++s) xa[s] = lds0 + (tb0 * 32 + r) * 128 + (((2 * s + h) ^ ((r >> 1) & 7)) << 4);
    unsigned wa[2];
    {   const int blk = (lane >> 4) & 1, q = (lane & 15) >> 2, p = lane & 3;
#pragma unroll
        for (int u = 0; u < 2; ++u) {
            const int row = 8 * h + 4 * u + q, ch = 4 * ctl + 2 * blk + (p >> 1);
            wa[u] = lds0 + 16384 + row * 256 + ((ch ^ (((row & 3) << 2) | ((row >> 2) & 3))) << 4) + 8 * (p & 1);
        } }
    f32x16 acc0 = {}, acc1 = {};
    KvRegs R;
    kv_issue(R, bk, bv, shift_k, kvtb, kvcg);
    issue(0); issue(1); issue(2); issue(3);
    const unsigned sc = lds0 + G1_SC;
    KV_WAIT(R, 16);
    kv_stage(R, kvtb, sc);
    struct Frags { bf16x8 w[4], x0[4], x1[4]; };
    Frags fa, fb;
    auto rd = [&](Frags& f, unsigned st, int s) {
        f.w[s] = cat8(trread(wa[0] + st + s * 4096), trread(wa[1] + st + s * 4096));
        f.x0[s] = ldsrd128(xa[s] + st); f.x1[s] = ldsrd128(xa[s] + st + 4096);
    };
    WAITBAR(12);
    if (active) { _Pragma("unroll") for (int s = 0; s < 4; ++s) rd(fa, 0, s); }
    kv_compute(R, sc);
    u32x4 fl;
    auto kloop = [&](auto isv) {
        constexpr bool V = decltype(isv)::value;
        auto mm = [&](const Frags& f, int s) {
            if (V) { acc0 = MFMA32(f.x0[s], f.w[s], acc0); acc1 = MFMA32(f.x1[s], f.w[s], acc1); }
            else   { acc0 = MFMA32(f.w[s], f.x0[s], acc0); acc1 = MFMA32(f.w[s], f.x1[s], acc1); }
        };
        auto body = [&](const Frags& cur, Frags& nxt, unsigned st) {
            if (active) {
#pragma unroll
                for (int s = 0; s < 4; ++s) { rd(nxt, st, s); SBAR(); mm(cur, s); SBAR(); }
            }
        };
        WAITBAR(8); issue(4); SBAR(); body(fa, fb, 1 * G1_STAGE);
        kv_store(rs, kvtb, kvcg, sc, w, lane);
        WAITBAR(9); issue(5); SBAR(); body(fb, fa, 2 * G1_STAGE);
        WAITBAR(9); issue(6); SBAR(); body(fa, fb, 3 * G1_STAGE);
        WAITBAR(9); issue(7); SBAR(); body(fb, fa, 0);
        WAITBAR(8); issue(8); SBAR();
        publish(rs, FL_KV + (kvcg * 32 + kvtb) * 4, epoch);
        body(fa, fb, 1 * G1_STAGE);
        WAITBAR(8); SBAR(); body(fb, fa, 2 * G1_STAGE);
        if (w == 0) WAITBAR(5); else WAITBAR(4);
        asm volatile("buffer_load_dwordx4 %0, %1, %2, 0 offen sc1" : "=v"(fl) : "v"(FL_KV + (pair >> 5) * 128 + (lane & 7) * 16), "s"(rs) : "memory");
        SBAR(); body(fa, fb, 3 * G1_STAGE);
        asm volatile("s_waitcnt vmcnt(0) lgkmcnt(0)\n\ts_barrier" : "+v"(fl) :: "memory");
        SBAR(); body(fb, fa, 0);
        if (active) { _Pragma("unroll") for (int s = 0; s < 4; ++s) mm(fa, s); }
    };
    if (ISV) kloop(BoolC<true>{}); else kloop(BoolC<false>{});
    STAMP(0, 1);
    if (w == 0) {
        const bool ok = fl[0] == epoch && fl[1] == epoch && fl[2] == epoch && fl[3] == epoch;
        if (!__all(ok)) poll_flags(rs, FL_KV + (pair >> 5) * 128, 32, epoch, lane);
    }
    LBAR();
    conv_issue(CR, ssm_k, rs, 2 * pair + (w >> 2), w);
    const int ct = ng * 4 + ctl;
    if (ISV) {
        if (active) {
#pragma unroll
            for (int i = 0; i < 2; ++i) {
                const f32x16& acc = i ? acc1 : acc0;
                const int T = mb * 4 + tb0 + i;
                const int col = (ct - 36) * 32 + r, head = col / 18, d = col - head * 18;
                const unsigned vp = (unsigned)WS_KV + head * HEAD_B + (((T >> 4) & 1) * 2 + (T >> 5)) * CH_B + CH_VT + (2 * (T & 15)) * 576 + h * 288 + d * 16;
#pragma unroll
                for (int g = 0; g < 2; ++g) {
                    const u32x4 o = {cvtpk(acc[8 * g], acc[8 * g + 1]), cvtpk(acc[8 * g + 2], acc[8 * g + 3]),
                                     cvtpk(acc[8 * g + 4], acc[8 * g + 5]), cvtpk(acc[8 * g + 6], acc[8 * g + 7])};
                    st16(rs, vp + g * 576, o);
                }
            }
        }
    } else {
        if (active) {
            const bool isq = ct < 18;
            const float scl = isq ? QSCALE : 1.0f;
            const int row = isq ? r : ((r & 16) | ((r & 4) << 1) | ((r & 8) >> 1) | (r & 3));
#pragma unroll
            for (int i = 0; i < 2; ++i) {
                const f32x16& acc = i ? acc1 : acc0;
#pragma unroll
                for (int g = 0; g < 4; ++g)
                    ldswr2(lds0 + (tb0 + i) * 8192 + (8 * ctl + 2 * g + h) * 256 + row * 8,
                           (u32x2){cvtpk(acc[4 * g] * scl, acc[4 * g + 1] * scl), cvtpk(acc[4 * g + 2] * scl, acc[4 * g + 3] * scl)});
            }
        }
        LBAR();
        const int g0 = ng * 32, blk0 = g0 / 9;
        u32x2 ca[5], cb[5]; unsigned cd[5]; bool c0[5], c1[5];
#pragma unroll
        for (int k5 = 0; k5 < 5; ++k5) {
            const int it = w + 8 * k5, tt = it / 10, cand = (it % 10) * 2 + h;
            const int blk = blk0 + (cand >> 2), cpos = cand & 3, gq = blk * 9 + (cpos & 1) * 2 + (cpos >> 1) * 5, lq = gq - g0;
            c0[k5] = lq >= 0 && lq < 32; c1[k5] = lq >= -1 && lq < 31;
            const bool isq = gq < 144;
            const int head = 2 * (isq ? blk : blk - 16) + (cpos >> 1), T = mb * 4 + tt;
            const unsigned qo = (unsigned)WS_Q + T * QT_B, ko = (unsigned)WS_KV + (((T >> 4) & 1) * 2 + (T >> 5)) * CH_B + (T & 15) * 1024;
            cd[k5] = (isq ? qo : ko) + head * (isq ? QH_B : HEAD_B) + (cpos & 1) * 512 + r * 16;
            const unsigned sa = lds0 + tt * 8192 + r * 8;
            ca[k5] = ldsrdu2(sa + (c0[k5] ? lq : 0) * 256); cb[k5] = ldsrdu2(sa + (c1[k5] ? lq + 1 : 0) * 256);
        }
        SBAR();
#pragma unroll
        for (int k5 = 0; k5 < 5; ++k5) {
            st16(rs, c0[k5] && c1[k5] ? cd[k5] : OOB, (u32x4){ca[k5][0], ca[k5][1], cb[k5][0], cb[k5][1]});
            if (__any(c0[k5] != c1[k5])) st8(rs, c0[k5] == c1[k5] ? OOB : c0[k5] ? cd[k5] : cd[k5] + 8, c0[k5] ? ca[k5] : cb[k5]);
        }
        {
            const int blk = blk0 + w, gq = blk * 9 + 4, lq = gq - g0;
            const bool ok = lq >= 0 && lq < 32;
            const int tt = lane >> 4, par = (lane >> 3) & 1, rq = lane & 7;
            const bool isq = gq < 144;
            const int head = 2 * (isq ? blk : blk - 16) + par, T = mb * 4 + tt;
            const unsigned sa = lds0 + tt * 8192 + (ok ? lq : 0) * 256 + rq * 32 + par * 4;
            const u32x4 v = {*(LAS const unsigned*)(uintptr_t)sa, *(LAS const unsigned*)(uintptr_t)(sa + 8),
                             *(LAS const unsigned*)(uintptr_t)(sa + 16), *(LAS const unsigned*)(uintptr_t)(sa + 24)};
            const unsigned qo = (unsigned)WS_Q + T * QT_B + 1024, ko = (unsigned)WS_KV + (((T >> 4) & 1) * 2 + (T >> 5)) * CH_B + CH_K2 + (T & 15) * 128;
            const unsigned dst = (isq ? qo : ko) + head * (isq ? QH_B : HEAD_B) + rq * 16;
            st16(rs, ok ? dst : OOB, v);
        }
    }
}

constexpr int CV_U = 33280, CV_B = CV_U + 7600;
constexpr int CV_BASE = LDS_CNT - 2 * CV_B;

__device__ __forceinline__ void conv_issue(ConvRegs& R, const u16* __restrict__ ssm_k, rsrc_t rs, int c, int w) {
    const int i = threadIdx.x & 255;
    const rsrc_t hr = __builtin_amdgcn_make_buffer_rsrc((void*)(ssm_k + (size_t)c * S), 0, S * 2, 0x00020000);
    const unsigned off = (unsigned)(((9 * i - 32) >> 1) * 4);
    if ((w & 3) == 0) {
        R.h0 = (u32x4){__builtin_amdgcn_raw_buffer_load_b32(hr, off, 0, 0), __builtin_amdgcn_raw_buffer_load_b32(hr, off + 4, 0, 0),
                       __builtin_amdgcn_raw_buffer_load_b32(hr, off + 8, 0, 0), __builtin_amdgcn_raw_buffer_load_b32(hr, off + 12, 0, 0)};
        R.h1 = (u32x4){__builtin_amdgcn_raw_buffer_load_b32(hr, off + 16, 0, 0), __builtin_amdgcn_raw_buffer_load_b32(hr, off + 20, 0, 0),
                       __builtin_amdgcn_raw_buffer_load_b32(hr, off + 24, 0, 0), __builtin_amdgcn_raw_buffer_load_b32(hr, off + 28, 0, 0)};
    } else {
        R.h0 = __builtin_amdgcn_raw_buffer_load_b128(hr, off, 0, 0);
        R.h1 = __builtin_amdgcn_raw_buffer_load_b128(hr, off + 16, 0, 0);
    }
    R.h2 = __builtin_amdgcn_raw_buffer_load_b32(hr, off + 32, 0, 0);
    R.u = ld16(rs, (unsigned)WS_KVT + (unsigned)c * (S * 2) + i * 16);
}
__device__ __forceinline__ void conv_build(const ConvRegs& R, unsigned base) {
    const int i = threadIdx.x & 255;
    if (i < 155) ldswr4(base + CV_U + i * 16, (u32x4){0u, 0u, 0u, 0u});
    if (i < 232) {
        const unsigned sh = (i & 1) * 16;
        const unsigned dw[9] = {R.h0[0], R.h0[1], R.h0[2], R.h0[3], R.h1[0], R.h1[1], R.h1[2], R.h1[3], R.h2};
        unsigned A[8], Bq[7];
#pragma unroll
        for (int n = 0; n < 8; ++n) A[n] = __builtin_amdgcn_alignbit(dw[n + 1], dw[n], sh);
#pragma unroll
        for (int n = 0; n < 7; ++n) Bq[n] = __builtin_amdgcn_alignbit(A[n + 1], A[n], 16);
        const unsigned a = base + 9 * i * 16;
        ldswr4(a, (u32x4){A[0], A[1], A[2], A[3]});
        if (i < 231) {
#pragma unroll
            for (int u = 1; u < 9; ++u) {
                if (u & 1) ldswr4(a + u * 16, (u32x4){Bq[u >> 1], Bq[(u >> 1) + 1], Bq[(u >> 1) + 2], Bq[(u >> 1) + 3]});
                else ldswr4(a + u * 16, (u32x4){A[u >> 1], A[(u >> 1) + 1], A[(u >> 1) + 2], A[(u >> 1) + 3]});
            }
        }
    }
    ldswr4(base + CV_U + (31 + (i >> 2)) * 80 + (i & 3) * 16, R.u);
}
template <int JB, int JE>
__device__ __forceinline__ void conv_range(unsigned aaddr, unsigned baddr, f32x16& d0, f32x16& d1) {
    auto boff = [](int j) { const int c2 = 1040 - 16 * j; return ((c2 >> 5) + 31) * 80 + (c2 & 16) * 2; };
    constexpr int PD = 4;
    bf16x8 a[PD], b1[PD], b0[PD];
#pragma unroll
    for (int j = JB; j < JB + PD; ++j) { a[(j - JB) % PD] = ldsrd128(aaddr + j * 256); b1[(j - JB) % PD] = ldsrd128(baddr + boff(j)); if (j < 64) b0[(j - JB) % PD] = ldsrd128(baddr + boff(j) - 2560); }
    SBAR();
#pragma unroll
    for (int j = JB; j < JE; ++j) {
        const int k = (j - JB) % PD;
        d1 = MFMA32(a[k], b1[k], d1);
        if (j < 64) d0 = MFMA32(a[k], b0[k], d0);
        SBAR();
        if (j + PD < JE) { a[k] = ldsrd128(aaddr + (j + PD) * 256); b1[k] = ldsrd128(baddr + boff(j + PD)); if (j + PD < 64) b0[k] = ldsrd128(baddr + boff(j + PD) - 2560); }
        SBAR();
    }
}
__device__ __forceinline__ void conv_main(unsigned base, unsigned lds0, int w, int lane, f32x16& d0, f32x16& d1, bool poll, int head, rsrc_t rs, unsigned epoch STAMP_ARG) {
    const int n = lane & 31, hh = lane >> 5, wg = w & 3;
    const unsigned aaddr = base + (n - 8 * hh + 9) * 16, baddr = base + CV_U + n * 80 + hh * 16;
    d0 = f32x16{}; d1 = f32x16{};
    if (wg == 0) conv_range<0, 27>(aaddr, baddr, d0, d1);
    else if (wg == 1) conv_range<27, 54>(aaddr, baddr, d0, d1);
    else if (wg == 2) conv_range<54, 98>(aaddr, baddr, d0, d1);
    else conv_range<98, 113>(aaddr, baddr, d0, d1);
    if (poll && w == 3) {
        const int e = lane >> 2, col = (e >> 1) * 576 + 18 * head + (e & 1) * 17;
        poll_at(rs, FL_QKV + blockIdx.x * 1024 + (col >> 7) * 64 + (lane & 3) * 16, lane < 24, epoch);
        STAMP(1, 15);
        asm volatile("buffer_inv sc1" ::: "memory");
    }
    if (wg == 3) conv_range<113, 128>(aaddr, baddr, d0, d1);
}
__device__ __forceinline__ void conv_partials(unsigned base, int wg, int lane, const f32x16& d0, const f32x16& d1) {
    const int n = lane & 31, hh = lane >> 5;
    const unsigned p = base + wg * 9216 + (36 * n + 4 * hh) * 4;
#pragma unroll
    for (int g = 0; g < 4; ++g) {
        if (wg < 3) *(LAS f32x4*)(uintptr_t)(p + g * 32) = (f32x4){d0[4 * g], d0[4 * g + 1], d0[4 * g + 2], d0[4 * g + 3]};
        *(LAS f32x4*)(uintptr_t)(p + 4608 + g * 32) = (f32x4){d1[4 * g], d1[4 * g + 1], d1[4 * g + 2], d1[4 * g + 3]};
    }
}
struct ConvOut { u32x4 z; };
__device__ __forceinline__ void conv_sum(ConvOut& co, unsigned base, const u16* __restrict__ ssm_D, int c, const u32x4& uu) {
    const int i = threadIdx.x & 255;
    const unsigned p = base + (8 * i + 4 * (i >> 2)) * 4;
    f32x4 lo = *(LAS const f32x4*)(uintptr_t)p, hi = *(LAS const f32x4*)(uintptr_t)(p + 16);
#pragma unroll
    for (int k = 1; k < 4; ++k)
        if (k < 3 || i >= 128) { lo += *(LAS const f32x4*)(uintptr_t)(p + k * 9216); hi += *(LAS const f32x4*)(uintptr_t)(p + k * 9216 + 16); }
    const float dd = bflo(ssm_D[c]);
    const float y[8] = {lo[0], lo[1], lo[2], lo[3], hi[0], hi[1], hi[2], hi[3]};
    unsigned o[4];
#pragma unroll
    for (int j = 0; j < 4; ++j) {
        const float u0 = bfhi(uu[3 - j]), u1 = bflo(uu[3 - j]);
        o[j] = cvtpk(rbf(y[2 * j]) + rbf(u0 * dd), rbf(y[2 * j + 1]) + rbf(u1 * dd));
    }
    co.z = (u32x4){o[0], o[1], o[2], o[3]};
}
__device__ __forceinline__ unsigned attn_tile_off(int t) { return t < 16 ? 0u : 2u * CH_B; }
__device__ __forceinline__ void attn_slow(unsigned base, int lane, bf16x8 qf, s16x4 q2, f32x16& o, float& mrow) {
    const int m_ = lane & 31, h = lane >> 5, vrow = m_ < 18 ? m_ : 0;
    const bool one = m_ == 18 || m_ == 22;
    const bf16x8 ones = __builtin_bit_cast(bf16x8, (u32x4){0x3f803f80u, 0x3f803f80u, 0x3f803f80u, 0x3f803f80u});
    float m = -3.0e38f;
    o = f32x16{};
    for (int t = 0; t < 32; ++t) {
        const unsigned cb = base + attn_tile_off(t), tl = t & 15;
        const bf16x8 kf = ldsrd128(cb + tl * 1024 + lane * 16);
        const unsigned k2 = *(LAS const unsigned*)(uintptr_t)(cb + CH_K2 + tl * 128 + m_ * 4);
        f32x16 s = {};
        s = MFMA32(kf, qf, s);
        s = __builtin_amdgcn_mfma_f32_32x32x8bf16_1k(__builtin_bit_cast(s16x4, (u32x2){k2, 0u}), q2, s, 0, 0, 0);
        float tm = s[0];
#pragma unroll
        for (int r = 1; r < 16; ++r) tm = fmaxf(tm, s[r]);
        tm = fmaxf(tm, __shfl_xor(tm, 32));
        const float mn = fmaxf(m, tm);
        const float f = __builtin_amdgcn_exp2f(m - mn);
        m = mn;
#pragma unroll
        for (int r = 0; r < 16; ++r) { o[r] *= f; s[r] = __builtin_amdgcn_exp2f(s[r] - mn); }
        const f32x8 lo = __builtin_shufflevector(s, s, 0, 1, 2, 3, 4, 5, 6, 7), hi = __builtin_shufflevector(s, s, 8, 9, 10, 11, 12, 13, 14, 15);
        const unsigned va = cb + CH_VT + tl * 1152 + h * 288 + vrow * 16;
        const bf16x8 v0 = one ? ones : ldsrd128(va), v1 = one ? ones : ldsrd128(va + 576);
        o = __builtin_amdgcn_smfmac_f32_32x32x32_bf16(v0, __builtin_bit_cast(bf16x16, lo), o, (int)0xDDDDDDDD, 0, 0);
        o = __builtin_amdgcn_smfmac_f32_32x32x32_bf16(v1, __builtin_bit_cast(bf16x16, hi), o, (int)0xDDDDDDDD, 0, 0);
    }
    mrow = m;
}
__device__ __forceinline__ bool attn_range_ok(const f32x16& o) {
    const float l = o[10];
    bool ok = (l >= 5.9604645e-8f) && (l <= 1.8446744e19f);
#pragma unroll
    for (int r = 0; r < 10; ++r) ok = ok && (fabsf(o[r]) <= 3.0e38f);
    return __all(ok);
}
template <int I0, int I1>
__device__ __forceinline__ void attn_dma(const char* ws, int head, int rot, unsigned lds0, int w, int lane) {
    const char* src = ws + WS_KV + (size_t)head * HEAD_B + lane * 16;
#pragma unroll
    for (int i = I0; i < I1; ++i) { int p = w + 8 * i + rot; p = p >= 72 ? p - 72 : p; glds16(src + p * 1024, lds0 + p * 1024); }
}
struct AttnQ { bf16x8 fA, fB; s16x4 rA, rB; unsigned a, b; };
__device__ __forceinline__ void attn_loadq(AttnQ& Q, const char* ws, int head, int qtA, int lane) {
    const char* qp = ws + WS_Q + (size_t)head * QH_B + (size_t)qtA * QT_B;
    asm volatile("global_load_dwordx4 %0, %1, off sc1" : "=v"(Q.fA) : "v"(qp + lane * 16) : "memory");
    asm volatile("global_load_dwordx4 %0, %1, off sc1" : "=v"(Q.fB) : "v"(qp + QT_B + lane * 16) : "memory");
    asm volatile("global_load_dword %0, %1, off sc1" : "=v"(Q.a) : "v"(qp + 1024 + (lane & 31) * 4) : "memory");
    asm volatile("global_load_dword %0, %1, off sc1" : "=v"(Q.b) : "v"(qp + QT_B + 1024 + (lane & 31) * 4) : "memory");
}
#define ATTN_QWAITBAR(Q, N) asm volatile("s_waitcnt vmcnt(" #N ") lgkmcnt(0)\n\ts_barrier" : "+v"(Q.fA), "+v"(Q.fB), "+v"(Q.a), "+v"(Q.b) :: "memory")
__device__ __forceinline__ void attn_fixq(AttnQ& Q, int lane) {
    Q.rA = __builtin_bit_cast(s16x4, (u32x2){lane < 32 ? Q.a : 0u, 0u}); Q.rB = __builtin_bit_cast(s16x4, (u32x2){lane < 32 ? Q.b : 0u, 0u});
}

__device__ __forceinline__ void attn_loop(AttnQ& Q, const char* ws, rsrc_t rs, int head, int rot, unsigned lds0, int w, int lane, f32x16& oA, f32x16& oB, u32x4& zf) {
    const int kpart = w >> 2, m = lane & 31, h = lane >> 5;
    const int vrow = m < 18 ? m : 0;
    unsigned ka = lds0 + kpart * CH_B + lane * 16, k2a = lds0 + kpart * CH_B + CH_K2 + m * 4, va = lds0 + kpart * CH_B + CH_VT + h * 288 + vrow * 16;
    unsigned vini = (m == 18 || m == 22) ? 0x3f803f80u : 0u;
    unsigned idx = 0xDDDDDDDDu;
    int cnt = 16;
    const unsigned long long vmask = ((1ull << 18) - 1) * 0x100000001ull;
    oA = f32x16{}; oB = f32x16{};
    const char* src2 = ws + WS_KV + (size_t)head * HEAD_B + 2 * CH_B;
    const unsigned zoff = FL_Z + lane * 16;
    unsigned lane16 = lane * 16, ldsb = lds0 + 2 * CH_B, pnext = w + rot >= 72 ? w + rot - 72 : w + rot;
#define EXP4(d0, d1, d2, d3, s0, s1, s2, s3) \
        "v_exp_f32 v" #d0 ", v" #s0 "\n v_exp_f32 v" #d1 ", v" #s1 "\n v_exp_f32 v" #d2 ", v" #s2 "\n v_exp_f32 v" #d3 ", v" #s3 "\n"
#define VRD(x) "s_mov_b64 exec, %13\n" x "s_mov_b64 exec, -1\n"
#define DMA2 "s_lshl_b32 s44, %11, 10\n s_add_u32 m0, %15, s44\n v_add_u32 v126, s44, %14\n s_add_u32 %11, %11, 8\n global_load_lds_dwordx4 v126, %16\n" \
             "s_cmp_ge_u32 %11, 72\n s_cselect_b32 s44, 72, 0\n s_sub_u32 %11, %11, s44\n"
#define INLOOP_DMA "s_cmp_lt_u32 %10, 12\n s_cbranch_scc1 3f\n" DMA2 DMA2 "3:\n"
#define MIDBAR "s_waitcnt vmcnt(0)\n s_barrier\n buffer_load_dwordx4 %12, %17, %18, 0 offen sc1\n"
#define KEYTILE(PK, OK, PK2, OK2, OVH, PV, OVL, PL, PH) \
        "v_mfma_f32_32x32x16_bf16 v[80:95], v[112:115], %3, 0\n" \
        PK "ds_read_b128 v[112:115], %7 offset:" #OK "\n" \
        PL EXP4(96, 97, 98, 99, 64, 65, 66, 67) PH \
        "v_mfma_f32_32x32x8_bf16 v[80:95], v[116:117], %5, v[80:95]\n" \
        PK2 "ds_read_b32 v116, %8 offset:" #OK2 "\n" \
        EXP4(100, 101, 102, 103, 68, 69, 70, 71) \
        "v_smfmac_f32_32x32x32_bf16 %1, v[122:125], v[104:111], %6\n" \
        VRD("ds_read_b128 v[122:125], %9 offset:" #OVH "\n") \
        EXP4(104, 105, 106, 107, 72, 73, 74, 75) \
        "s_waitcnt lgkmcnt(3)\n" \
        "v_smfmac_f32_32x32x32_bf16 %0, v[118:121], v[96:103], %6\n" \
        EXP4(108, 109, 110, 111, 76, 77, 78, 79) \
        "s_waitcnt lgkmcnt(2)\n" \
        "v_mfma_f32_32x32x16_bf16 v[64:79], v[112:115], %2, 0\n" \
        PL EXP4(96, 97, 98, 99, 80, 81, 82, 83) PH \
        "s_waitcnt lgkmcnt(1)\n" \
        "v_mfma_f32_32x32x8_bf16 v[64:79], v[116:117], %4, v[64:79]\n" \
        EXP4(100, 101, 102, 103, 84, 85, 86, 87) \
        "s_waitcnt lgkmcnt(0)\n" \
        "v_smfmac_f32_32x32x32_bf16 %0, v[122:125], v[104:111], %6\n" \
        EXP4(104, 105, 106, 107, 88, 89, 90, 91) \
        "v_smfmac_f32_32x32x32_bf16 %1, v[118:121], v[96:103], %6\n" \
        PV VRD("ds_read_b128 v[118:121], %9 offset:" #OVL "\n") \
        EXP4(108, 109, 110, 111, 92, 93, 94, 95)
#define LOOPASM(PL, PH, PRE) \
    asm volatile( \
        PRE \
        "ds_read_b128 v[112:115], %7\n" \
        "ds_read_b32 v116, %8\n" \
        "v_mov_b32 v117, 0\n" \
        "v_mov_b32 v104, 0\n v_mov_b32 v105, 0\n v_mov_b32 v106, 0\n v_mov_b32 v107, 0\n" \
        "v_mov_b32 v108, 0\n v_mov_b32 v109, 0\n v_mov_b32 v110, 0\n v_mov_b32 v111, 0\n" \
        "v_mov_b32 v122, %19\n v_mov_b32 v123, %19\n v_mov_b32 v124, %19\n v_mov_b32 v125, %19\n" \
        "v_mov_b32 v118, %19\n v_mov_b32 v119, %19\n v_mov_b32 v120, %19\n v_mov_b32 v121, %19\n" \
        "s_waitcnt lgkmcnt(0)\n" \
        "v_mfma_f32_32x32x16_bf16 v[64:79], v[112:115], %2, 0\n" \
        "v_mfma_f32_32x32x8_bf16 v[64:79], v[116:117], %4, v[64:79]\n" \
        VRD("ds_read_b128 v[118:121], %9\n") \
        "s_nop 7\n s_nop 7\n" \
        "1:\n" \
        INLOOP_DMA \
        "s_movk_i32 s40, 2048\n s_movk_i32 s41, 256\n s_movk_i32 s42, 2304\n" \
        "s_cmp_lg_u32 %10, 9\n s_cbranch_scc1 2f\n" \
        MIDBAR \
        "s_mov_b32 s40, 59392\n s_mov_b32 s41, 71936\n s_mov_b32 s42, 57600\n" \
        "2:\n" \
        KEYTILE("", 1024, "", 128, 576, "", 1152, PL, PH) \
        KEYTILE("v_add_u32 %7, s40, %7\n", 0, "v_add_u32 %8, s41, %8\n", 0, 1728, "v_add_u32 %9, s42, %9\n", 0, PL, PH) \
        "s_sub_u32 %10, %10, 1\n s_cmp_lg_u32 %10, 0\n s_cbranch_scc1 1b\n" \
        "s_waitcnt vmcnt(0) lgkmcnt(0)\n" \
        "v_smfmac_f32_32x32x32_bf16 %1, v[122:125], v[104:111], %6\n" \
        "s_setprio 0\n s_nop 7\n s_nop 7\n" \
        : "+v"(oA), "+v"(oB), "+v"(Q.fA), "+v"(Q.fB), "+v"(Q.rA), "+v"(Q.rB), "+v"(idx), "+v"(ka), "+v"(k2a), "+v"(va), "+s"(cnt), "+s"(pnext), "=&v"(zf) \
        : "s"(vmask), "v"(lane16), "s"(ldsb), "s"(src2), "v"(zoff), "s"(rs), "v"(vini) \
        : "v64", "v65", "v66", "v67", "v68", "v69", "v70", "v71", "v72", "v73", "v74", "v75", "v76", "v77", "v78", "v79", \
          "v80", "v81", "v82", "v83", "v84", "v85", "v86", "v87", "v88", "v89", "v90", "v91", "v92", "v93", "v94", "v95", \
          "v96", "v97", "v98", "v99", "v100", "v101", "v102", "v103", "v104", "v105", "v106", "v107", "v108", "v109", "v110", "v111", \
          "v112", "v113", "v114", "v115", "v116", "v117", "v118", "v119", "v120", "v121", "v122", "v123", "v124", "v125", \
          "v126", "s40", "s41", "s42", "s44", "scc", "memory")
    if (w < 4) { LOOPASM("s_setprio 0\n", "s_setprio 2\n", "s_setprio 2\n"); } else { LOOPASM("", "", "s_setprio 1\n"); }
#undef LOOPASM
#undef KEYTILE
#undef MIDBAR
#undef INLOOP_DMA
#undef DMA2
#undef VRD
#undef EXP4
}
struct AttnM { float a, b; };
__device__ __forceinline__ void attn_check(AttnQ& Q, f32x16& oA, f32x16& oB, AttnM& M, unsigned lds0, int w, int lane) {
    const int kpart = w >> 2;
    M.a = 0.f; M.b = 0.f;
    const bool okA = attn_range_ok(oA), okB = attn_range_ok(oB);
    if (!(okA && okB)) {
#pragma unroll 1
        for (int i = 0; i < 2; ++i) {
            if (i ? okB : okA) continue;
            f32x16 o; float mr;
            attn_slow(lds0 + kpart * CH_B, lane, i ? Q.fB : Q.fA, i ? Q.rB : Q.rA, o, mr);
            if (i) { oB = o; M.b = mr; } else { oA = o; M.a = mr; }
        }
    }
}
__device__ __forceinline__ void attn_give(const f32x16& oA, const f32x16& oB, const AttnM& M, unsigned lds0, int w, int lane) {
    const int kpart = w >> 2;
    LAS float* xch = (LAS float*)(uintptr_t)(lds0 + w * 3072);
#pragma unroll
    for (int r = 0; r < 11; ++r) xch[r * 64 + lane] = kpart ? oA[r] : oB[r];
    xch[11 * 64 + lane] = kpart ? M.a : M.b;
}
__device__ __forceinline__ void attn_store(const f32x16& oA, const f32x16& oB, const AttnM& M, rsrc_t rs, int head, int qtA, unsigned lds0, int w, int lane) {
    const int kpart = w >> 2, m = lane & 31, h = lane >> 5;
    LAS const float* xin = (LAS const float*)(uintptr_t)(lds0 + (w ^ 4) * 3072);
    const float m0 = kpart ? M.b : M.a, m1 = xin[11 * 64 + lane];
    const float mm = fmaxf(m0, m1), f0 = __builtin_amdgcn_exp2f(m0 - mm), f1 = __builtin_amdgcn_exp2f(m1 - mm);
    float v[11];
#pragma unroll
    for (int r = 0; r < 11; ++r) v[r] = (kpart ? oB[r] : oA[r]) * f0 + xin[r * 64 + lane] * f1;
    const float inv = 1.0f / v[10];
    const int tb = qtA + kpart;
    const u32x4 o8 = {cvtpk(v[0] * inv, v[1] * inv), cvtpk(v[2] * inv, v[3] * inv), cvtpk(v[4] * inv, v[5] * inv), cvtpk(v[6] * inv, v[7] * inv)};
    st16(rs, (unsigned)WS_OM + ((2 * tb + (m >> 4)) * 16 + (head >> 1)) * 1024 + ((2 * (head & 1) + h) * 16 + (m & 15)) * 16, o8);
    const unsigned rr = cvtpk(v[8] * inv, v[9] * inv);
    const unsigned r0 = __shfl(rr, (lane & 7) * 4), r1 = __shfl(rr, (lane & 7) * 4 + 1), r2 = __shfl(rr, (lane & 7) * 4 + 2), r3 = __shfl(rr, (lane & 7) * 4 + 3);
    st16(rs, lane < 8 ? (unsigned)WS_OR + (tb * 32 + head) * 128 + lane * 16 : OOB, (u32x4){r0, r1, r2, r3});
}

constexpr int P4_SLAB = 24576, P4_ZT = P4_SLAB + 576 * 144, P4_RED = P4_ZT + 72 * 144;
__device__ __forceinline__ void proj_dma(const u16* __restrict__ wo, int cg, int rot, unsigned lds0, int w, int lane) {
#pragma unroll
    for (int i = 0; i < 11; ++i) {
        int p = w + 8 * i + rot; p = p >= 81 ? p - 81 : p; p = p >= 81 ? p - 81 : p;
        const int ck = p * 64 + lane, row = ck / 9, c9 = ck - row * 9;
        glds16(wo + (size_t)row * D + cg * 72 + c9 * 8, lds0 + P4_SLAB + p * 1024);
    }
}
struct GateRegs { u32x4 z0, z1, q0, q1; };
__device__ __forceinline__ void gate_issue(GateRegs& R, rsrc_t rs, const u16* __restrict__ bq, int tb, int cg, int w, int lane) {
    const int tid = threadIdx.x;
    R.z0 = ld16(rs, (unsigned)WS_ZT + (unsigned)(cg * 72 + (tid >> 3)) * (S * 2) + (tb * 64 + (tid & 7) * 8) * 2);
    R.z1 = R.z0;
    if (tid < 64) R.z1 = ld16(rs, (unsigned)WS_ZT + (unsigned)(cg * 72 + 64 + (tid >> 3)) * (S * 2) + (tb * 64 + (tid & 7) * 8) * 2);
    const int G = lane >> 4;
    { const int u = 4 * w + G, tg = u / 9, cb = u - tg * 9; R.q0 = *(const u32x4*)(bq + (size_t)(tb * 64 + tg * 16 + (lane & 15)) * D + cg * 72 + cb * 8); }
    R.q1 = R.q0;
    if (w == 0) { const int u = 32 + G, tg = u / 9, cb = u - tg * 9; R.q1 = *(const u32x4*)(bq + (size_t)(tb * 64 + tg * 16 + (lane & 15)) * D + cg * 72 + cb * 8); }
}
__device__ __forceinline__ void gate_stage(const GateRegs& R, unsigned lds0) {
    const int tid = threadIdx.x;
    ldswr4(lds0 + P4_ZT + (tid >> 3) * 144 + (tid & 7) * 16, R.z0);
    if (tid < 64) ldswr4(lds0 + P4_ZT + (64 + (tid >> 3)) * 144 + (tid & 7) * 16, R.z1);
}
__device__ __forceinline__ void gate_unit(unsigned lds0, u16* __restrict__ out, const u32x4& qq, int u, int tb, int cg, int lane) {
    const int tg = u / 9, cb = u - tg * 9, q = (lane & 15) >> 2, p = lane & 3;
    const unsigned a = lds0 + P4_ZT + (8 * cb + q) * 144 + (16 * tg + 4 * p) * 2;
    const u32x2 zl = __builtin_bit_cast(u32x2, trread(a)), zh = __builtin_bit_cast(u32x2, trread(a + 4 * 144));
    const u32x4 r = {cvtpk(bflo(qq[0]) * bflo(zl[0]), bfhi(qq[0]) * bfhi(zl[0])), cvtpk(bflo(qq[1]) * bflo(zl[1]), bfhi(qq[1]) * bfhi(zl[1])),
                     cvtpk(bflo(qq[2]) * bflo(zh[0]), bfhi(qq[2]) * bfhi(zh[0])), cvtpk(bflo(qq[3]) * bflo(zh[1]), bfhi(qq[3]) * bfhi(zh[1]))};
    *(u32x4*)(out + (size_t)(tb * 64 + tg * 16 + (lane & 15)) * OUTW + D + cg * 72 + cb * 8) = r;
}
__device__ __forceinline__ void gate_finish(const GateRegs& R, unsigned lds0, u16* out, int tb, int cg, int w, int lane) {
    gate_unit(lds0, out, R.q0, 4 * w + (lane >> 4), tb, cg, lane);
    if (w == 0) gate_unit(lds0, out, R.q1, 32 + (lane >> 4), tb, cg, lane);
}
struct ProjW { bf16x8 f[9][5]; };
__device__ __forceinline__ void proj_readw(ProjW& W, unsigned lds0, int w, int lane) {
    const int kh = w >> 2, kq = lane >> 4, q = (lane & 15) >> 2, p = lane & 3;
#pragma unroll
    for (int s = 0; s < 9; ++s) {
        const int st = kh * 9 + s;
        int r0, r1;
        if (st < 16) { const int hd = 2 * st + (kq >> 1), hh = kq & 1; r0 = 18 * hd + 4 * hh + q; r1 = r0 + 8; }
        else { const int hd = 16 * (st - 16) + 4 * kq + (q >> 1); r0 = 18 * hd + 16 + (q & 1); r1 = r0 + 36; }
#pragma unroll
        for (int c = 0; c < 5; ++c) {
            const int cc = (c == 4 && p >= 2) ? 64 + (p & 1) * 4 : 16 * c + 4 * p;
            W.f[s][c] = cat8(trread(lds0 + P4_SLAB + r0 * 144 + cc * 2), trread(lds0 + P4_SLAB + r1 * 144 + cc * 2));
        }
    }
}
__device__ __forceinline__ void proj_tile(const ProjW& W, rsrc_t rs, u16* __restrict__ out, int tb64, int cg, unsigned lds0, int w, int lane) {
    const int tt = w & 3, kh = w >> 2, t16 = tb64 * 4 + tt, kq = lane >> 4;
    bf16x8 af[9];
#pragma unroll
    for (int s = 0; s < 9; ++s) {
        const int st = kh * 9 + s;
        if (st < 16) af[s] = __builtin_bit_cast(bf16x8, ld16(rs, (unsigned)WS_OM + (t16 * 16 + st) * 1024 + lane * 16));
        else {
            const unsigned ro = (unsigned)WS_OR + ((t16 >> 1) * 32 + 16 * (st - 16) + 4 * (lane >> 4)) * 128 + ((t16 & 1) * 16 + (lane & 15)) * 4;
            af[s] = __builtin_bit_cast(bf16x8, (u32x4){ld4(rs, ro), ld4(rs, ro + 128), ld4(rs, ro + 256), ld4(rs, ro + 384)});
        }
    }
    f32x4 acc[5] = {};
#pragma unroll
    for (int s = 0; s < 9; ++s)
#pragma unroll
        for (int c = 0; c < 5; ++c) acc[c] = MFMA16(W.f[s][c], af[s], acc[c]);
    LAS f32x4* red = (LAS f32x4*)(uintptr_t)(lds0 + P4_RED + tt * 5120);
    if (kh == 1) {
#pragma unroll
        for (int c = 0; c < 5; ++c) red[c * 64 + lane] = acc[c];
    }
    LBAR();
    if (kh == 0) {
        u16* op = out + (size_t)(t16 * 16 + (lane & 15)) * OUTW + cg * 72 + 4 * kq;
#pragma unroll
        for (int c = 0; c < 5; ++c) {
            const f32x4 v = acc[c] + red[c * 64 + lane];
            if (c < 4 || kq < 2) *(u32x2*)(op + 16 * c) = (u32x2){cvtpk(v[0], v[1]), cvtpk(v[2], v[3])};
        }
    }
}

__global__ __launch_bounds__(512, 2) void fused(const u16* __restrict__ x, const u16* __restrict__ wqkv, const u16* __restrict__ wo,
                                                const u16* __restrict__ bq, const u16* __restrict__ bk, const u16* __restrict__ bv,
                                                const u16* __restrict__ shift_k, const u16* __restrict__ ssm_k, const u16* __restrict__ ssm_D,
                                                char* __restrict__ ws, u16* __restrict__ out) {
    extern __shared__ __attribute__((aligned(16))) char lds[];
    const unsigned lds0 = (unsigned)(uintptr_t)lds;
    const int lane = threadIdx.x & 63, w = __builtin_amdgcn_readfirstlane(threadIdx.x >> 6), b = blockIdx.x;
    const rsrc_t rs = ws_rsrc(ws);
    STAMP_DECL;
    STAMP(0, 0);
    const unsigned epoch = *(const __attribute__((address_space(4))) unsigned*)(uintptr_t)ws + 1u;
    if (threadIdx.x == 0) ldswr1(lds0 + LDS_CNT, 0u);
    const bool gemm = b < 224;
    ConvRegs CR;
    const int mb = ((b & 7) >> 1) * 4 + ((b >> 3) & 3), ng = (b & 1) * 7 + (b >> 5);
    if (gemm) {
        qkv_tile(ng >= 9, x, wqkv, rs, mb, ng, lds0, w, lane, bk, bv, shift_k, b & 31, b >> 5, epoch, CR, ssm_k, b STAMP_PASS);
    } else kv_only(bk, bv, shift_k, rs, lds0, w, lane, epoch);
    STAMP(0, 2);
    const int head = (b & 7) * 4 + ((b >> 3) & 3), qb = b >> 5, qtA = qb * 8 + (w & 3) * 2;
    AttnQ Q;
    const int rounds = gemm ? 1 : 2, pair0 = gemm ? b : 224 + 2 * (b - 224);
#pragma unroll 1
    for (int rd = 0; rd < rounds; ++rd) {
        const int pair = pair0 + rd, c = 2 * pair + (w >> 2);
        const bool last = rd + 1 == rounds;
        const unsigned base = lds0 + CV_BASE + (w >> 2) * CV_B;
        if (!gemm) {
            if (w == 0) poll_flags(rs, FL_KV + (pair >> 5) * 128, 32, epoch, lane);
            LBAR();
            conv_issue(CR, ssm_k, rs, c, w);
        }
        conv_build(CR, base);
        WAITBAR(0);
        if (gemm && w == 0) publish_qkv(rs, mb, ng, epoch, lane);
        STAMP(1, 3);
        f32x16 d0, d1;
        conv_main(base, lds0, w, lane, d0, d1, last, head, rs, epoch STAMP_PASS);
        STAMP(1, 4);
        WAITBAR(0);
        STAMP(1, 5);
        if (last) { attn_loadq(Q, ws, head, qtA, lane); attn_dma<0, 3>(ws, head, 9 * qb, lds0, w, lane); }
        conv_partials(base, w & 3, lane, d0, d1);
        if (last) attn_dma<3, 6>(ws, head, 9 * qb, lds0, w, lane);
        LBAR();
        STAMP(1, 6);
        ConvOut co;
        conv_sum(co, base, ssm_D, c, CR.u);
        if (last) attn_dma<6, 9>(ws, head, 9 * qb, lds0, w, lane);
        st16(rs, (unsigned)WS_ZT + (unsigned)c * (S * 2) + (threadIdx.x & 255) * 16, co.z);
    }
    STAMP(1, 7);
    ATTN_QWAITBAR(Q, 0);
    publish(rs, FL_Z + b * 4, epoch);
    attn_fixq(Q, lane);
    STAMP(1, 8);
    f32x16 oA, oB;
    u32x4 zf;
    attn_loop(Q, ws, rs, head, 9 * qb, lds0, w, lane, oA, oB, zf);
    STAMP(1, 9);
    if (!__all(zf[0] == epoch && zf[1] == epoch && zf[2] == epoch && zf[3] == epoch)) poll_flags(rs, FL_Z, 256, epoch, lane);
    GateRegs G;
    gate_issue(G, rs, bq, b >> 3, b & 7, w, lane);
    AttnM M;
    attn_check(Q, oA, oB, M, lds0, w, lane);
    LBAR();
    attn_give(oA, oB, M, lds0, w, lane);
    LBAR();
    attn_store(oA, oB, M, rs, head, qtA, lds0, w, lane);
    STAMP(1, 10);
    const int i5 = b & 31, ptb = (b >> 5) * 4 + ((i5 & 7) >> 1), pcg = (i5 & 1) * 4 + (i5 >> 3);
    proj_dma(wo, pcg, 10 * (b >> 5), lds0, w, lane);
    WAITBAR(11);
    if (w == 0) st4(rs, lane < 32 ? FL_O + ((b & ~31) + lane) * 128 + (b & 31) * 4 : OOB, epoch);
    STAMP(1, 11);
    gate_stage(G, lds0);
    if (w == 0) WAITBAR(1); else WAITBAR(0);
    gate_finish(G, lds0, out, b >> 3, b & 7, w, lane);
    ProjW W;
    proj_readw(W, lds0, w, lane);
    if (w == 0) poll_at(rs, FL_O + b * 128 + lane * 16, lane < 8, epoch);
    LBAR();
    STAMP(1, 13);
    proj_tile(W, rs, out, ptb, pcg, lds0, w, lane);
    if (b == 0 && threadIdx.x == 0) *(unsigned*)ws = epoch;
    STAMP(1, 14);
    STAMP_FLUSH(0);
}

extern "C" void kernel_launch(void* const* d_in, const int* in_sizes, int n_in, void* d_out, int out_size, void* d_ws, size_t ws_size,
                              hipStream_t stream) {
    (void)hipFuncSetAttribute((const void*)fused, hipFuncAttributeMaxDynamicSharedMemorySize, LDS_BYTES);
    fused<<<256, 512, LDS_BYTES, stream>>>((const u16*)d_in[0], (const u16*)d_in[1], (const u16*)d_in[2], (const u16*)d_in[3], (const u16*)d_in[4],
                                           (const u16*)d_in[5], (const u16*)d_in[6], (const u16*)d_in[7], (const u16*)d_in[8], (char*)d_ws, (u16*)d_out);
}
```

```cpp
#include <hip/hip_runtime.h>
#include <hip/hip_bf16.h>
#include <stdint.h>

#define LAS __attribute__((address_space(3)))
typedef __attribute__((ext_vector_type(8))) __bf16 bf16x8;
typedef __attribute__((ext_vector_type(16))) __bf16 bf16x16;
typedef __attribute__((ext_vector_type(4))) short s16x4;
typedef __attribute__((ext_vector_type(8))) short s16x8;
typedef __attribute__((ext_vector_type(16))) float f32x16;
typedef __attribute__((ext_vector_type(8))) float f32x8;
typedef __attribute__((ext_vector_type(4))) float f32x4;
typedef __attribute__((ext_vector_type(4))) unsigned u32x4;
typedef __attribute__((ext_vector_type(2))) unsigned u32x2;
typedef unsigned short u16;

constexpr int S = 2048, D = 576, NH = 32;
constexpr int OUTW = 2 * D;
constexpr int CH_K2 = 16384, CH_VT = 18432, CH_B = 36864, HEAD_B = 4 * CH_B;
constexpr int QT_B = 1152, QH_B = 64 * QT_B;
constexpr size_t WS_FLAG = 0;
constexpr unsigned FL_GEN = 0, FL_KV = 1024, FL_Z = 3584, FL_O = 8192, FL_QKV = FL_O + 256 * 128;
constexpr size_t WS_KV = FL_QKV + 256 * 1024;
constexpr size_t WS_Q = WS_KV + (size_t)NH * HEAD_B;
constexpr size_t WS_OM = WS_Q + (size_t)NH * QH_B;
constexpr size_t WS_OR = WS_OM + (size_t)128 * 16 * 1024;
constexpr size_t WS_KVT = WS_OR + (size_t)64 * 32 * 128;
constexpr size_t WS_ZT = WS_KVT + (size_t)S * D * 2;
constexpr size_t WS_END = WS_ZT + (size_t)S * D * 2;
constexpr float QSCALE = 0.23570226039551584f * 1.4426950408889634f;
constexpr int LDS_CNT = 158720, LDS_BYTES = LDS_CNT + 64;

#define STAMP_DECL
#define STAMP(kid, pt)
#define STAMP_FLUSH(kid)
#define STAMP_ARG
#define STAMP_PASS
#define GST(pt)
#define SBAR() __builtin_amdgcn_sched_barrier(0)
#define MFMA32(a, b, c) __builtin_amdgcn_mfma_f32_32x32x16_bf16(a, b, c, 0, 0, 0)
#define MFMA16(a, b, c) __builtin_amdgcn_mfma_f32_16x16x32_bf16(a, b, c, 0, 0, 0)
#define WAITBAR(V) asm volatile("s_waitcnt vmcnt(" #V ") lgkmcnt(0)\n\ts_barrier" ::: "memory")
#define LBAR() asm volatile("s_waitcnt lgkmcnt(0)\n\ts_barrier" ::: "memory")

__device__ __forceinline__ void glds16(const void* g, unsigned lds_base) {
    asm volatile("s_mov_b32 m0, %1\n\ts_nop 0\n\tglobal_load_lds_dwordx4 %0, off" :: "v"(g), "s"(lds_base) : "memory");
}
__device__ __forceinline__ unsigned cvtpk(float lo, float hi) {
    typedef __attribute__((ext_vector_type(2))) float f2; typedef __attribute__((ext_vector_type(2))) __bf16 b2;
    f2 v = {lo, hi};
    return __builtin_bit_cast(unsigned, __builtin_convertvector(v, b2));
}
__device__ __forceinline__ float bflo(unsigned u) { return __uint_as_float(u << 16); }
__device__ __forceinline__ float bfhi(unsigned u) { return __uint_as_float(u & 0xffff0000u); }
__device__ __forceinline__ float rbf(float x) { return bflo(cvtpk(x, 0.f)); }
__device__ __forceinline__ s16x4 trread(unsigned a) {
    return __builtin_bit_cast(s16x4, __builtin_amdgcn_ds_read_tr16_b64_v4i16((LAS s16x4*)(uintptr_t)a));
}
__device__ __forceinline__ bf16x8 cat8(s16x4 a, s16x4 b) { return __builtin_bit_cast(bf16x8, (s16x8)__builtin_shufflevector(a, b, 0, 1, 2, 3, 4, 5, 6, 7)); }
__device__ __forceinline__ bf16x8 ldsrd128(unsigned a) { return *(LAS const bf16x8*)(uintptr_t)a; }
__device__ __forceinline__ u32x4 ldsrdu4(unsigned a) { return *(LAS const u32x4*)(uintptr_t)a; }
__device__ __forceinline__ u32x2 ldsrdu2(unsigned a) { return *(LAS const u32x2*)(uintptr_t)a; }
__device__ __forceinline__ void ldswr4(unsigned a, u32x4 v) { *(LAS u32x4*)(uintptr_t)a = v; }
__device__ __forceinline__ void ldswr2(unsigned a, u32x2 v) { *(LAS u32x2*)(uintptr_t)a = v; }
__device__ __forceinline__ void ldswr1(unsigned a, unsigned v) { *(LAS unsigned*)(uintptr_t)a = v; }

__device__ __forceinline__ unsigned l2touch(const void* p) { unsigned r; asm volatile("global_load_dword %0, %1, off" : "=v"(r) : "v"(p) : "memory"); return r; }
#define KEEP(x) asm volatile("" :: "v"(x))

typedef __amdgpu_buffer_rsrc_t rsrc_t;
__device__ __forceinline__ rsrc_t ws_rsrc(char* ws) { return __builtin_amdgcn_make_buffer_rsrc(ws, 0, (int)WS_END, 0x00020000); }
__device__ __forceinline__ void st16(rsrc_t r, unsigned off, u32x4 v) { __builtin_amdgcn_raw_buffer_store_b128(v, r, off, 0, 16); }
__device__ __forceinline__ void st8(rsrc_t r, unsigned off, u32x2 v) { __builtin_amdgcn_raw_buffer_store_b64(v, r, off, 0, 16); }
__device__ __forceinline__ void st4(rsrc_t r, unsigned off, unsigned v) { __builtin_amdgcn_raw_buffer_store_b32(v, r, off, 0, 16); }
__device__ __forceinline__ u32x4 ld16(rsrc_t r, unsigned off) { return __builtin_amdgcn_raw_buffer_load_b128(r, off, 0, 16); }
__device__ __forceinline__ unsigned ld4(rsrc_t r, unsigned off) { return __builtin_amdgcn_raw_buffer_load_b32(r, off, 0, 16); }

__device__ __forceinline__ void publish(rsrc_t rs, unsigned off, unsigned epoch) { if (threadIdx.x == 0) st4(rs, off, epoch); }
__device__ __forceinline__ void publish_qkv(rsrc_t rs, int mb, int ng, unsigned epoch, int lane) {
    const int c0 = ng * 128 - (ng >= 9 ? 1152 : ng >= 5 ? 576 : 0), h0 = c0 / 18 , h1 = min((c0 + 127) / 18, 31);
#pragma unroll
    for (int k = 0; k < 2; ++k) {
        const int l = lane + 64 * k, hi = l >> 3, qb = l & 7;
        int head = h0 + hi; bool ok = head <= h1;
        if (ng == 4) { head = (28 + hi) & 31; ok = hi < 8; }
        const int consumer = (qb << 5) | ((head & 3) << 3) | (head >> 2);
        st4(rs, ok ? FL_QKV + consumer * 1024 + (ng * 16 + mb) * 4 : 0xFFFFFF00u, epoch);
    }
}
__device__ __forceinline__ void poll_at(rsrc_t rs, unsigned off, bool act, unsigned epoch) {
#pragma unroll 1
    for (int spin = 0; spin < (1 << 18); ++spin) {
        bool ok = true;
        if (act) { const u32x4 v = ld16(rs, off); ok = v[0] == epoch && v[1] == epoch && v[2] == epoch && v[3] == epoch; }
        if (__all(ok)) break;
        __builtin_amdgcn_s_sleep(2);
        asm volatile("" ::: "memory");
    }
    asm volatile("" ::: "memory");
}
__device__ __forceinline__ void poll_flags(rsrc_t rs, unsigned off, int nwords, unsigned epoch, int lane) {
#pragma unroll 1
    for (int spin = 0; spin < (1 << 18); ++spin) {
        bool ok = true;
        if (lane * 4 < nwords) { const u32x4 v = ld16(rs, off + lane * 16); ok = v[0] == epoch && v[1] == epoch && v[2] == epoch && v[3] == epoch; }
        if (__all(ok)) break;
        __builtin_amdgcn_s_sleep(4);
        asm volatile("" ::: "memory");
    }
    asm volatile("" ::: "memory");
}

constexpr int KVS_K = 0, KVS_W = 67 * 144, KVS_T = KVS_W + 512, KVS_B = KVS_T + 64 * 144;
struct KvRegs { u32x4 k, v, x; };
__device__ __forceinline__ u32x4 gload16(const void* p) {
    u32x4 r; asm volatile("global_load_dwordx4 %0, %1, off" : "=v"(r) : "v"(p) : "memory"); return r;
}
__device__ __forceinline__ void kv_issue(KvRegs& R, const u16* __restrict__ bk, const u16* __restrict__ bv, const u16* __restrict__ shift_k, int tb, int cg) {
    const int tid = threadIdx.x, t = tb * 64 + (tid >> 3), c = cg * 64 + (tid & 7) * 8;
    R.k = gload16(bk + (size_t)t * D + c);
    R.v = gload16(bv + (size_t)t * D + c);
    const u16* xp = bk + c;
    if (tid < 24) { const int th = tb * 64 - 3 + (tid >> 3); xp = bk + (size_t)(th < 0 ? 0 : th) * D + c; }
    else if (tid >= 64 && tid < 96) xp = shift_k + cg * 256 + (tid - 64) * 8;
    R.x = gload16(xp);
}
#define KV_WAIT(R, N) asm volatile("s_waitcnt vmcnt(" #N ")" : "+v"(R.k), "+v"(R.v), "+v"(R.x) :: "memory")
__device__ __forceinline__ void kv_stage(const KvRegs& R, int tb, unsigned sc) {
    const int tid = threadIdx.x;
    ldswr4(sc + KVS_K + ((tid >> 3) + 3) * 144 + (tid & 7) * 16, R.k);
    if (tid < 24) ldswr4(sc + KVS_K + (tid >> 3) * 144 + (tid & 7) * 16, (tb * 64 - 3 + (tid >> 3) < 0) ? (u32x4){0u, 0u, 0u, 0u} : R.x);
    else if (tid >= 64 && tid < 96) ldswr4(sc + KVS_W + (tid - 64) * 16, R.x);
}
__device__ __forceinline__ float dot2bf(unsigned a, unsigned b, float c) {
    typedef __attribute__((ext_vector_type(2))) __bf16 b2;
    return __builtin_amdgcn_fdot2_f32_bf16(__builtin_bit_cast(b2, a), __builtin_bit_cast(b2, b), c, false);
}
__device__ __forceinline__ unsigned kv_pair(unsigned k0, unsigned k1, unsigned k2, unsigned k3, unsigned vv, unsigned wa0, unsigned wa1, unsigned wb0, unsigned wb1) {
    const float sl = dot2bf(__builtin_amdgcn_perm(k2, k3, 0x05040100u), wa0, dot2bf(__builtin_amdgcn_perm(k0, k1, 0x05040100u), wa1, 0.f));
    const float sh = dot2bf(__builtin_amdgcn_perm(k2, k3, 0x07060302u), wb0, dot2bf(__builtin_amdgcn_perm(k0, k1, 0x07060302u), wb1, 0.f));
    const unsigned s = cvtpk(sl, sh);
    return cvtpk(bflo(s) * bflo(vv), bfhi(s) * bfhi(vv));
}
__device__ __forceinline__ void kv_compute(const KvRegs& R, unsigned sc) {
    const int tid = threadIdx.x, trow = tid >> 3, ch = tid & 7;
    const unsigned ka = sc + KVS_K + trow * 144 + ch * 16;
    const u32x4 k0 = ldsrdu4(ka), k1 = ldsrdu4(ka + 144), k2 = ldsrdu4(ka + 288);
    const unsigned wa = sc + KVS_W + ch * 64;
    const u32x4 w0 = ldsrdu4(wa), w1 = ldsrdu4(wa + 16), w2 = ldsrdu4(wa + 32), w3 = ldsrdu4(wa + 48);
    const u32x4 r = {kv_pair(k0[0], k1[0], k2[0], R.k[0], R.v[0], w0[0], w0[1], w0[2], w0[3]),
                     kv_pair(k0[1], k1[1], k2[1], R.k[1], R.v[1], w1[0], w1[1], w1[2], w1[3]),
                     kv_pair(k0[2], k1[2], k2[2], R.k[2], R.v[2], w2[0], w2[1], w2[2], w2[3]),
                     kv_pair(k0[3], k1[3], k2[3], R.k[3], R.v[3], w3[0], w3[1], w3[2], w3[3])};
    const int row = (trow & ~7) | (7 - (trow & 7));
    ldswr4(sc + KVS_T + row * 144 + ch * 16, r);
}
__device__ __forceinline__ void kv_store(rsrc_t rs, int tb, int cg, unsigned sc, int w, int lane) {
    const int G = lane >> 4, q = (lane & 15) >> 2, p = lane & 3;
    const int cw = (w >> 1) * 16;
    s16x4 a[4];
#pragma unroll
    for (int u = 0; u < 4; ++u) a[u] = trread(sc + KVS_T + (16 * G + 4 * u + q) * 144 + (cw + 4 * p) * 2);
    const int i = lane & 15;
    if ((i >> 3) == (w & 1)) {
        const unsigned off = (unsigned)WS_KVT + (unsigned)(cg * 64 + cw + i) * (S * 2) + (tb * 64 + 16 * G) * 2;
        st16(rs, off, __builtin_bit_cast(u32x4, (s16x8)__builtin_shufflevector(a[0], a[1], 0, 1, 2, 3, 4, 5, 6, 7)));
        st16(rs, off + 16, __builtin_bit_cast(u32x4, (s16x8)__builtin_shufflevector(a[2], a[3], 0, 1, 2, 3, 4, 5, 6, 7)));
    }
}

template <bool B> struct BoolC { static constexpr bool value = B; };
constexpr int G1_STAGE = 32768, G1_SC = 4 * G1_STAGE;
constexpr unsigned OOB = 0xFFFFFF00u;

struct ConvRegs { u32x4 h0, h1; unsigned h2; u32x4 u; };
__device__ __forceinline__ void conv_issue(ConvRegs& R, const u16* __restrict__ ssm_k, rsrc_t rs, int c, int w);

__device__ __forceinline__ void qkv_tile(const bool ISV, const u16* __restrict__ x, const u16* __restrict__ wqkv, rsrc_t rs,
                                         int mb, int ng, int rot, unsigned lds0, int w, int lane,
                                         const u16* __restrict__ bk, const u16* __restrict__ bv, const u16* __restrict__ shift_k, int kvtb, int kvcg,
                                         unsigned epoch, ConvRegs& CR, const u16* __restrict__ ssm_k, int pair STAMP_ARG) {
    const int ctl = w & 3, tb0 = (w >> 2) * 2;
    const bool active = (ng * 4 + ctl) < 54;
    const u16* xsrc[2]; const u16* wsrc[2];
#pragma unroll
    for (int i = 0; i < 2; ++i) {
        const int p = w * 2 + i;
        const int xr = 8 * p + (lane >> 3), xc = (lane & 7) ^ ((xr >> 1) & 7);
        xsrc[i] = x + (size_t)(mb * 128 + xr) * D + xc * 8;
        const int wr = 4 * p + (lane >> 4), wc = (lane & 15) ^ (((wr & 3) << 2) | ((wr >> 2) & 3));
        int col = ng * 128 + wc * 8; col = col > 1720 ? 1720 : col;
        wsrc[i] = wqkv + (size_t)wr * (3 * D) + col;
    }
    int kc = rot;
    auto issue = [&](unsigned slot) {
        const int xo = kc * 64, wo_ = kc * 64 * (3 * D);
#pragma unroll
        for (int i = 0; i < 2; ++i) {
            glds16(xsrc[i] + xo, lds0 + slot + (w * 2 + i) * 1024);
            glds16(wsrc[i] + wo_, lds0 + slot + 16384 + (w * 2 + i) * 1024);
        }
        kc = kc == 8 ? 0 : kc + 1;
    };
    const int r = lane & 31, h = lane >> 5;
    unsigned xa[4];
#pragma unroll
    for (int s = 0; s < 4; ++s) xa[s] = lds0 + (tb0 * 32 + r) * 128 + (((2 * s + h) ^ ((r >> 1) & 7)) << 4);
    unsigned wa[2];
    {   const int blk = (lane >> 4) & 1, q = (lane & 15) >> 2, p = lane & 3;
#pragma unroll
        for (int u = 0; u < 2; ++u) {
            const int row = 8 * h + 4 * u + q, ch = 4 * ctl + 2 * blk + (p >> 1);
            wa[u] = lds0 + 16384 + row * 256 + ((ch ^ (((row & 3) << 2) | ((row >> 2) & 3))) << 4) + 8 * (p & 1);
        } }
    f32x16 acc0 = {}, acc1 = {};
    KvRegs R;
    issue(0);
    kv_issue(R, bk, bv, shift_k, kvtb, kvcg);
    issue(G1_STAGE);
    struct Frags { bf16x8 w[4], x0[4], x1[4]; };
    Frags fa, fb;
    auto rd = [&](Frags& f, unsigned st, int s) {
        f.w[s] = cat8(trread(wa[0] + st + s * 4096), trread(wa[1] + st + s * 4096));
        f.x0[s] = ldsrd128(xa[s] + st); f.x1[s] = ldsrd128(xa[s] + st + 4096);
    };
    const unsigned sc = lds0 + G1_SC;
    KV_WAIT(R, 4);
    kv_stage(R, kvtb, sc);
    WAITBAR(4);
    if (active) { _Pragma("unroll") for (int s = 0; s < 4; ++s) rd(fa, 0, s); }
    issue(2 * G1_STAGE);
    kv_compute(R, sc);
    u32x4 fl;
    auto kloop = [&](auto isv) {
        constexpr bool V = decltype(isv)::value;
        auto mm = [&](const Frags& f, int s) {
            if (V) { acc0 = MFMA32(f.x0[s], f.w[s], acc0); acc1 = MFMA32(f.x1[s], f.w[s], acc1); }
            else   { acc0 = MFMA32(f.w[s], f.x0[s], acc0); acc1 = MFMA32(f.w[s], f.x1[s], acc1); }
        };
        auto body = [&](const Frags& cur, Frags& nxt, unsigned st) {
            if (active) {
#pragma unroll
                for (int s = 0; s < 4; ++s) { rd(nxt, st, s); SBAR(); mm(cur, s); SBAR(); }
            }
        };
        WAITBAR(4);  issue(3 * G1_STAGE); issue(0); SBAR(); body(fa, fb, 1 * G1_STAGE);
        kv_store(rs, kvtb, kvcg, sc, w, lane);
        WAITBAR(10); issue(1 * G1_STAGE); SBAR(); body(fb, fa, 2 * G1_STAGE);
        WAITBAR(10); issue(2 * G1_STAGE); SBAR(); body(fa, fb, 3 * G1_STAGE);
        WAITBAR(10); issue(3 * G1_STAGE); SBAR(); body(fb, fa, 0);
        WAITBAR(8);
        publish(rs, FL_KV + (kvcg * 32 + kvtb) * 4, epoch);
        issue(0); SBAR(); body(fa, fb, 1 * G1_STAGE);
        WAITBAR(8);  SBAR(); body(fb, fa, 2 * G1_STAGE);
        if (w == 0) WAITBAR(5); else WAITBAR(4);
        asm volatile("buffer_load_dwordx4 %0, %1, %2, 0 offen sc1" : "=v"(fl) : "v"(FL_KV + (pair >> 5) * 128 + (lane & 7) * 16), "s"(rs) : "memory");
        SBAR(); body(fa, fb, 3 * G1_STAGE);
        asm volatile("s_waitcnt vmcnt(0) lgkmcnt(0)\n\ts_barrier" : "+v"(fl) :: "memory");
        SBAR(); body(fb, fa, 0);
        if (active) { _Pragma("unroll") for (int s = 0; s < 4; ++s) mm(fa, s); }
    };
    if (ISV) kloop(BoolC<true>{}); else kloop(BoolC<false>{});
    STAMP(0, 1);
    if (w == 0) {
        const bool ok = fl[0] == epoch && fl[1] == epoch && fl[2] == epoch && fl[3] == epoch;
        if (!__all(ok)) poll_flags(rs, FL_KV + (pair >> 5) * 128, 32, epoch, lane);
    }
    LBAR();
    conv_issue(CR, ssm_k, rs, 2 * pair + (w >> 2), w);
    const int ct = ng * 4 + ctl;
    if (ISV) {
        if (active) {
#pragma unroll
            for (int i = 0; i < 2; ++i) {
                const f32x16& acc = i ? acc1 : acc0;
                const int T = mb * 4 + tb0 + i;
                const int col = (ct - 36) * 32 + r, head = col / 18, d = col - head * 18;
                const unsigned vp = (unsigned)WS_KV + head * HEAD_B + (((T >> 4) & 1) * 2 + (T >> 5)) * CH_B + CH_VT + (2 * (T & 15)) * 576 + h * 288 + d * 16;
#pragma unroll
                for (int g = 0; g < 2; ++g) {
                    const u32x4 o = {cvtpk(acc[8 * g], acc[8 * g + 1]), cvtpk(acc[8 * g + 2], acc[8 * g + 3]),
                                     cvtpk(acc[8 * g + 4], acc[8 * g + 5]), cvtpk(acc[8 * g + 6], acc[8 * g + 7])};
                    st16(rs, vp + g * 576, o);
                }
            }
        }
    } else {
        if (active) {
            const bool isq = ct < 18;
            const float scl = isq ? QSCALE : 1.0f;
            const int row = isq ? r : ((r & 16) | ((r & 4) << 1) | ((r & 8) >> 1) | (r & 3));
#pragma unroll
            for (int i = 0; i < 2; ++i) {
                const f32x16& acc = i ? acc1 : acc0;
#pragma unroll
                for (int g = 0; g < 4; ++g)
                    ldswr2(lds0 + (tb0 + i) * 8192 + (8 * ctl + 2 * g + h) * 256 + row * 8,
                           (u32x2){cvtpk(acc[4 * g] * scl, acc[4 * g + 1] * scl), cvtpk(acc[4 * g + 2] * scl, acc[4 * g + 3] * scl)});
            }
        }
        LBAR();
        const int g0 = ng * 32, blk0 = g0 / 9;
#pragma unroll
        for (int k5 = 0; k5 < 5; ++k5) {
            const int it = w + 8 * k5, tt = it / 10, cand = (it % 10) * 2 + h;
            const int blk = blk0 + (cand >> 2), cpos = cand & 3, gq = blk * 9 + (cpos & 1) * 2 + (cpos >> 1) * 5, lq = gq - g0;
            const bool in0 = lq >= 0 && lq < 32, in1 = lq >= -1 && lq < 31;
            const bool isq = gq < 144;
            const int head = 2 * (isq ? blk : blk - 16) + (cpos >> 1), T = mb * 4 + tt;
            const unsigned dst = (isq ? (unsigned)WS_Q + head * QH_B + T * QT_B
                                      : (unsigned)WS_KV + head * HEAD_B + (((T >> 4) & 1) * 2 + (T >> 5)) * CH_B + (T & 15) * 1024) + (cpos & 1) * 512 + r * 16;
            const unsigned sa = lds0 + tt * 8192 + r * 8;
            const u32x2 a = ldsrdu2(sa + (in0 ? lq : 0) * 256), b = ldsrdu2(sa + (in1 ? lq + 1 : 0) * 256);
            st16(rs, in0 && in1 ? dst : OOB, (u32x4){a[0], a[1], b[0], b[1]});
            if (__any(in0 != in1)) st8(rs, in0 == in1 ? OOB : in0 ? dst : dst + 8, in0 ? a : b);
        }
        {
            const int blk = blk0 + w, gq = blk * 9 + 4, lq = gq - g0;
            const bool ok = lq >= 0 && lq < 32;
            const int tt = lane >> 4, par = (lane >> 3) & 1, rq = lane & 7;
            const bool isq = gq < 144;
            const int head = 2 * (isq ? blk : blk - 16) + par, T = mb * 4 + tt;
            const unsigned sa = lds0 + tt * 8192 + (ok ? lq : 0) * 256 + rq * 32 + par * 4;
            const u32x4 v = {*(LAS const unsigned*)(uintptr_t)sa, *(LAS const unsigned*)(uintptr_t)(sa + 8),
                             *(LAS const unsigned*)(uintptr_t)(sa + 16), *(LAS const unsigned*)(uintptr_t)(sa + 24)};
            const unsigned dst = (isq ? (unsigned)WS_Q + head * QH_B + T * QT_B + 1024
                                      : (unsigned)WS_KV + head * HEAD_B + (((T >> 4) & 1) * 2 + (T >> 5)) * CH_B + CH_K2 + (T & 15) * 128) + rq * 16;
            st16(rs, ok ? dst : OOB, v);
        }
    }
}

__device__ __forceinline__ void kv_only(const u16* bk, const u16* bv, const u16* shift_k, rsrc_t rs, unsigned lds0, int w, int lane, unsigned epoch) {
    const int t = 224 + ((int)blockIdx.x - 224) * 2;
    KvRegs R0, R1;
    kv_issue(R0, bk, bv, shift_k, t & 31, t >> 5); kv_issue(R1, bk, bv, shift_k, (t + 1) & 31, (t + 1) >> 5);
    KV_WAIT(R0, 3); kv_stage(R0, t & 31, lds0);
    KV_WAIT(R1, 0); kv_stage(R1, (t + 1) & 31, lds0 + KVS_B);
    LBAR();
    kv_compute(R0, lds0); kv_compute(R1, lds0 + KVS_B);
    LBAR();
    kv_store(rs, t & 31, t >> 5, lds0, w, lane); kv_store(rs, (t + 1) & 31, (t + 1) >> 5, lds0 + KVS_B, w, lane);
    WAITBAR(0);
    publish(rs, FL_KV + t * 4, epoch);
    if (threadIdx.x == 0) st4(rs, FL_KV + (t + 1) * 4, epoch);
}

constexpr int CV_U = 33280, CV_B = CV_U + 7600;
constexpr int CV_BASE = LDS_CNT - 2 * CV_B;

__device__ __forceinline__ void conv_issue(ConvRegs& R, const u16* __restrict__ ssm_k, rsrc_t rs, int c, int w) {
    const int i = threadIdx.x & 255;
    const rsrc_t hr = __builtin_amdgcn_make_buffer_rsrc((void*)(ssm_k + (size_t)c * S), 0, S * 2, 0x00020000);
    const unsigned off = (unsigned)(((9 * i - 32) >> 1) * 4);
    if ((w & 3) == 0) {
        R.h0 = (u32x4){__builtin_amdgcn_raw_buffer_load_b32(hr, off, 0, 0), __builtin_amdgcn_raw_buffer_load_b32(hr, off + 4, 0, 0),
                       __builtin_amdgcn_raw_buffer_load_b32(hr, off + 8, 0, 0), __builtin_amdgcn_raw_buffer_load_b32(hr, off + 12, 0, 0)};
        R.h1 = (u32x4){__builtin_amdgcn_raw_buffer_load_b32(hr, off + 16, 0, 0), __builtin_amdgcn_raw_buffer_load_b32(hr, off + 20, 0, 0),
                       __builtin_amdgcn_raw_buffer_load_b32(hr, off + 24, 0, 0), __builtin_amdgcn_raw_buffer_load_b32(hr, off + 28, 0, 0)};
    } else {
        R.h0 = __builtin_amdgcn_raw_buffer_load_b128(hr, off, 0, 0);
        R.h1 = __builtin_amdgcn_raw_buffer_load_b128(hr, off + 16, 0, 0);
    }
    R.h2 = __builtin_amdgcn_raw_buffer_load_b32(hr, off + 32, 0, 0);
    R.u = ld16(rs, (unsigned)WS_KVT + (unsigned)c * (S * 2) + i * 16);
}
__device__ __forceinline__ void conv_build(const ConvRegs& R, unsigned base) {
    const int i = threadIdx.x & 255;
    if (i < 155) ldswr4(base + CV_U + i * 16, (u32x4){0u, 0u, 0u, 0u});
    if (i < 232) {
        const unsigned sh = (i & 1) * 16;
        const unsigned dw[9] = {R.h0[0], R.h0[1], R.h0[2], R.h0[3], R.h1[0], R.h1[1], R.h1[2], R.h1[3], R.h2};
        unsigned A[8], Bq[7];
#pragma unroll
        for (int n = 0; n < 8; ++n) A[n] = __builtin_amdgcn_alignbit(dw[n + 1], dw[n], sh);
#pragma unroll
        for (int n = 0; n < 7; ++n) Bq[n] = __builtin_amdgcn_alignbit(A[n + 1], A[n], 16);
        const unsigned a = base + 9 * i * 16;
        ldswr4(a, (u32x4){A[0], A[1], A[2], A[3]});
        if (i < 231) {
#pragma unroll
            for (int u = 1; u < 9; ++u) {
                if (u & 1) ldswr4(a + u * 16, (u32x4){Bq[u >> 1], Bq[(u >> 1) + 1], Bq[(u >> 1) + 2], Bq[(u >> 1) + 3]});
                else ldswr4(a + u * 16, (u32x4){A[u >> 1], A[(u >> 1) + 1], A[(u >> 1) + 2], A[(u >> 1) + 3]});
            }
        }
    }
    ldswr4(base + CV_U + (31 + (i >> 2)) * 80 + (i & 3) * 16, R.u);
}
template <int JB, int JE>
__device__ __forceinline__ void conv_range(unsigned aaddr, unsigned baddr, f32x16& d0, f32x16& d1) {
    auto boff = [](int j) { const int c2 = 1040 - 16 * j; return ((c2 >> 5) + 31) * 80 + (c2 & 16) * 2; };
    constexpr int PD = 4;
    bf16x8 a[PD], b1[PD], b0[PD];
#pragma unroll
    for (int j = JB; j < JB + PD; ++j) { a[(j - JB) % PD] = ldsrd128(aaddr + j * 256); b1[(j - JB) % PD] = ldsrd128(baddr + boff(j)); if (j < 64) b0[(j - JB) % PD] = ldsrd128(baddr + boff(j) - 2560); }
    SBAR();
#pragma unroll
    for (int j = JB; j < JE; ++j) {
        const int k = (j - JB) % PD;
        d1 = MFMA32(a[k], b1[k], d1);
        if (j < 64) d0 = MFMA32(a[k], b0[k], d0);
        SBAR();
        if (j + PD < JE) { a[k] = ldsrd128(aaddr + (j + PD) * 256); b1[k] = ldsrd128(baddr + boff(j + PD)); if (j + PD < 64) b0[k] = ldsrd128(baddr + boff(j + PD) - 2560); }
        SBAR();
    }
}
__device__ __forceinline__ void conv_main(unsigned base, unsigned lds0, int w, int lane, f32x16& d0, f32x16& d1, bool poll, int head, rsrc_t rs, unsigned epoch STAMP_ARG) {
    const int n = lane & 31, hh = lane >> 5, wg = w & 3;
    const unsigned aaddr = base + (n - 8 * hh + 9) * 16, baddr = base + CV_U + n * 80 + hh * 16;
    d0 = f32x16{}; d1 = f32x16{};
    if (wg == 0) conv_range<0, 13>(aaddr, baddr, d0, d1);
    else if (wg == 1) conv_range<25, 38>(aaddr, baddr, d0, d1);
    else if (wg == 2) conv_range<50, 64>(aaddr, baddr, d0, d1);
    else conv_range<86, 108>(aaddr, baddr, d0, d1);
    if (poll && w == 3) {
        const int e = lane >> 2, col = (e >> 1) * 576 + 18 * head + (e & 1) * 17;
        poll_at(rs, FL_QKV + blockIdx.x * 1024 + (col >> 7) * 64 + (lane & 3) * 16, lane < 24, epoch);
        STAMP(1, 15);
        asm volatile("buffer_inv sc1" ::: "memory");
    }
    if (wg == 0) conv_range<13, 25>(aaddr, baddr, d0, d1);
    else if (wg == 1) conv_range<38, 50>(aaddr, baddr, d0, d1);
    else if (wg == 2) conv_range<64, 86>(aaddr, baddr, d0, d1);
    else conv_range<108, 128>(aaddr, baddr, d0, d1);
}
__device__ __forceinline__ void conv_partials(unsigned base, int wg, int lane, const f32x16& d0, const f32x16& d1) {
    const int n = lane & 31, hh = lane >> 5;
    const unsigned p = base + wg * 9216 + (36 * n + 4 * hh) * 4;
#pragma unroll
    for (int g = 0; g < 4; ++g) {
        if (wg < 3) *(LAS f32x4*)(uintptr_t)(p + g * 32) = (f32x4){d0[4 * g], d0[4 * g + 1], d0[4 * g + 2], d0[4 * g + 3]};
        *(LAS f32x4*)(uintptr_t)(p + 4608 + g * 32) = (f32x4){d1[4 * g], d1[4 * g + 1], d1[4 * g + 2], d1[4 * g + 3]};
    }
}
struct ConvOut { u32x4 z; };
__device__ __forceinline__ void conv_sum(ConvOut& co, unsigned base, const u16* __restrict__ ssm_D, int c, const u32x4& uu) {
    const int i = threadIdx.x & 255;
    const unsigned p = base + (8 * i + 4 * (i >> 2)) * 4;
    f32x4 lo = *(LAS const f32x4*)(uintptr_t)p, hi = *(LAS const f32x4*)(uintptr_t)(p + 16);
#pragma unroll
    for (int k = 1; k < 4; ++k)
        if (k < 3 || i >= 128) { lo += *(LAS const f32x4*)(uintptr_t)(p + k * 9216); hi += *(LAS const f32x4*)(uintptr_t)(p + k * 9216 + 16); }
    const float dd = bflo(ssm_D[c]);
    const float y[8] = {lo[0], lo[1], lo[2], lo[3], hi[0], hi[1], hi[2], hi[3]};
    unsigned o[4];
#pragma unroll
    for (int j = 0; j < 4; ++j) {
        const float u0 = bfhi(uu[3 - j]), u1 = bflo(uu[3 - j]);
        o[j] = cvtpk(rbf(y[2 * j]) + rbf(u0 * dd), rbf(y[2 * j + 1]) + rbf(u1 * dd));
    }
    co.z = (u32x4){o[0], o[1], o[2], o[3]};
}
__device__ __forceinline__ unsigned attn_tile_off(int t) { return t < 16 ? 0u : 2u * CH_B; }
__device__ __forceinline__ void attn_slow(unsigned base, int lane, bf16x8 qf, s16x4 q2, f32x16& o, float& mrow) {
    const int m_ = lane & 31, h = lane >> 5, vrow = m_ < 18 ? m_ : 0;
    const bool one = m_ == 18 || m_ == 22;
    const bf16x8 ones = __builtin_bit_cast(bf16x8, (u32x4){0x3f803f80u, 0x3f803f80u, 0x3f803f80u, 0x3f803f80u});
    float m = -3.0e38f;
    o = f32x16{};
    for (int t = 0; t < 32; ++t) {
        const unsigned cb = base + attn_tile_off(t), tl = t & 15;
        const bf16x8 kf = ldsrd128(cb + tl * 1024 + lane * 16);
        const unsigned k2 = *(LAS const unsigned*)(uintptr_t)(cb + CH_K2 + tl * 128 + m_ * 4);
        f32x16 s = {};
        s = MFMA32(kf, qf, s);
        s = __builtin_amdgcn_mfma_f32_32x32x8bf16_1k(__builtin_bit_cast(s16x4, (u32x2){k2, 0u}), q2, s, 0, 0, 0);
        float tm = s[0];
#pragma unroll
        for (int r = 1; r < 16; ++r) tm = fmaxf(tm, s[r]);
        tm = fmaxf(tm, __shfl_xor(tm, 32));
        const float mn = fmaxf(m, tm);
        const float f = __builtin_amdgcn_exp2f(m - mn);
        m = mn;
#pragma unroll
        for (int r = 0; r < 16; ++r) { o[r] *= f; s[r] = __builtin_amdgcn_exp2f(s[r] - mn); }
        const f32x8 lo = __builtin_shufflevector(s, s, 0, 1, 2, 3, 4, 5, 6, 7), hi = __builtin_shufflevector(s, s, 8, 9, 10, 11, 12, 13, 14, 15);
        const unsigned va = cb + CH_VT + tl * 1152 + h * 288 + vrow * 16;
        const bf16x8 v0 = one ? ones : ldsrd128(va), v1 = one ? ones : ldsrd128(va + 576);
        o = __builtin_amdgcn_smfmac_f32_32x32x32_bf16(v0, __builtin_bit_cast(bf16x16, lo), o, (int)0xDDDDDDDD, 0, 0);
        o = __builtin_amdgcn_smfmac_f32_32x32x32_bf16(v1, __builtin_bit_cast(bf16x16, hi), o, (int)0xDDDDDDDD, 0, 0);
    }
    mrow = m;
}
__device__ __forceinline__ bool attn_range_ok(const f32x16& o) {
    const float l = o[10];
    bool ok = (l >= 5.9604645e-8f) && (l <= 1.8446744e19f);
#pragma unroll
    for (int r = 0; r < 10; ++r) ok = ok && (fabsf(o[r]) <= 3.0e38f);
    return __all(ok);
}
template <int I0, int I1>
__device__ __forceinline__ void attn_dma(const char* ws, int head, int rot, unsigned lds0, int w, int lane) {
    const char* src = ws + WS_KV + (size_t)head * HEAD_B + lane * 16;
#pragma unroll
    for (int i = I0; i < I1; ++i) { int p = w + 8 * i + rot; p = p >= 72 ? p - 72 : p; glds16(src + p * 1024, lds0 + p * 1024); }
}
struct AttnQ { bf16x8 fA, fB; s16x4 rA, rB; unsigned a, b; };
__device__ __forceinline__ void attn_loadq(AttnQ& Q, const char* ws, int head, int qtA, int lane) {
    const char* qp = ws + WS_Q + (size_t)head * QH_B + (size_t)qtA * QT_B;
    asm volatile("global_load_dwordx4 %0, %1, off sc1" : "=v"(Q.fA) : "v"(qp + lane * 16) : "memory");
    asm volatile("global_load_dwordx4 %0, %1, off sc1" : "=v"(Q.fB) : "v"(qp + QT_B + lane * 16) : "memory");
    asm volatile("global_load_dword %0, %1, off sc1" : "=v"(Q.a) : "v"(qp + 1024 + (lane & 31) * 4) : "memory");
    asm volatile("global_load_dword %0, %1, off sc1" : "=v"(Q.b) : "v"(qp + QT_B + 1024 + (lane & 31) * 4) : "memory");
}
#define ATTN_QWAITBAR(Q, N) asm volatile("s_waitcnt vmcnt(" #N ") lgkmcnt(0)\n\ts_barrier" : "+v"(Q.fA), "+v"(Q.fB), "+v"(Q.a), "+v"(Q.b) :: "memory")
__device__ __forceinline__ void attn_fixq(AttnQ& Q, int lane) {
    Q.rA = __builtin_bit_cast(s16x4, (u32x2){lane < 32 ? Q.a : 0u, 0u}); Q.rB = __builtin_bit_cast(s16x4, (u32x2){lane < 32 ? Q.b : 0u, 0u});
}

__device__ __forceinline__ void attn_loop(AttnQ& Q, const char* ws, rsrc_t rs, int head, int rot, unsigned lds0, int w, int lane, f32x16& oA, f32x16& oB, u32x4& zf) {
    const int kpart = w >> 2, m = lane & 31, h = lane >> 5;
    const int vrow = m < 18 ? m : 0;
    unsigned ka = lds0 + kpart * CH_B + lane * 16, k2a = lds0 + kpart * CH_B + CH_K2 + m * 4, va = lds0 + kpart * CH_B + CH_VT + h * 288 + vrow * 16;
    unsigned vini = (m == 18 || m == 22) ? 0x3f803f80u : 0u;
    unsigned idx = 0xDDDDDDDDu;
    int cnt = 16;
    const unsigned long long vmask = ((1ull << 18) - 1) * 0x100000001ull;
    oA = f32x16{}; oB = f32x16{};
    const char* src2 = ws + WS_KV + (size_t)head * HEAD_B + 2 * CH_B;
    const unsigned zoff = FL_Z + lane * 16;
    unsigned lane16 = lane * 16, ldsb = lds0 + 2 * CH_B, pnext = w + rot >= 72 ? w + rot - 72 : w + rot;
#define EXP4(d0, d1, d2, d3, s0, s1, s2, s3) \
        "v_exp_f32 v" #d0 ", v" #s0 "\n v_exp_f32 v" #d1 ", v" #s1 "\n v_exp_f32 v" #d2 ", v" #s2 "\n v_exp_f32 v" #d3 ", v" #s3 "\n"
#define VRD(x) "s_mov_b64 exec, %13\n" x "s_mov_b64 exec, -1\n"
#define DMA2 "s_lshl_b32 s44, %11, 10\n s_add_u32 m0, %15, s44\n v_add_u32 v126, s44, %14\n s_add_u32 %11, %11, 8\n global_load_lds_dwordx4 v126, %16\n" \
             "s_cmp_ge_u32 %11, 72\n s_cselect_b32 s44, 72, 0\n s_sub_u32 %11, %11, s44\n"
#define INLOOP_DMA "s_cmp_lt_u32 %10, 12\n s_cbranch_scc1 3f\n" DMA2 DMA2 "3:\n"
#define MIDBAR "s_waitcnt vmcnt(0)\n s_barrier\n buffer_load_dwordx4 %12, %17, %18, 0 offen sc1\n"
#define KEYTILE(PK, OK, PK2, OK2, OVH, PV, OVL, PL, PH) \
        "v_mfma_f32_32x32x16_bf16 v[80:95], v[112:115], %3, 0\n" \
        PK "ds_read_b128 v[112:115], %7 offset:" #OK "\n" \
        PL EXP4(96, 97, 98, 99, 64, 65, 66, 67) PH \
        "v_mfma_f32_32x32x8_bf16 v[80:95], v[116:117], %5, v[80:95]\n" \
        PK2 "ds_read_b32 v116, %8 offset:" #OK2 "\n" \
        EXP4(100, 101, 102, 103, 68, 69, 70, 71) \
        "v_smfmac_f32_32x32x32_bf16 %1, v[122:125], v[104:111], %6\n" \
        VRD("ds_read_b128 v[122:125], %9 offset:" #OVH "\n") \
        EXP4(104, 105, 106, 107, 72, 73, 74, 75) \
        "s_waitcnt lgkmcnt(3)\n" \
        "v_smfmac_f32_32x32x32_bf16 %0, v[118:121], v[96:103], %6\n" \
        EXP4(108, 109, 110, 111, 76, 77, 78, 79) \
        "s_waitcnt lgkmcnt(2)\n" \
        "v_mfma_f32_32x32x16_bf16 v[64:79], v[112:115], %2, 0\n" \
        PL EXP4(96, 97, 98, 99, 80, 81, 82, 83) PH \
        "s_waitcnt lgkmcnt(1)\n" \
        "v_mfma_f32_32x32x8_bf16 v[64:79], v[116:117], %4, v[64:79]\n" \
        EXP4(100, 101, 102, 103, 84, 85, 86, 87) \
        "s_waitcnt lgkmcnt(0)\n" \
        "v_smfmac_f32_32x32x32_bf16 %0, v[122:125], v[104:111], %6\n" \
        EXP4(104, 105, 106, 107, 88, 89, 90, 91) \
        "v_smfmac_f32_32x32x32_bf16 %1, v[118:121], v[96:103], %6\n" \
        PV VRD("ds_read_b128 v[118:121], %9 offset:" #OVL "\n") \
        EXP4(108, 109, 110, 111, 92, 93, 94, 95)
#define LOOPASM(PL, PH, PRE) \
    asm volatile( \
        PRE \
        "ds_read_b128 v[112:115], %7\n" \
        "ds_read_b32 v116, %8\n" \
        "v_mov_b32 v117, 0\n" \
        "v_mov_b32 v104, 0\n v_mov_b32 v105, 0\n v_mov_b32 v106, 0\n v_mov_b32 v107, 0\n" \
        "v_mov_b32 v108, 0\n v_mov_b32 v109, 0\n v_mov_b32 v110, 0\n v_mov_b32 v111, 0\n" \
        "v_mov_b32 v122, %19\n v_mov_b32 v123, %19\n v_mov_b32 v124, %19\n v_mov_b32 v125, %19\n" \
        "v_mov_b32 v118, %19\n v_mov_b32 v119, %19\n v_mov_b32 v120, %19\n v_mov_b32 v121, %19\n" \
        "s_waitcnt lgkmcnt(0)\n" \
        "v_mfma_f32_32x32x16_bf16 v[64:79], v[112:115], %2, 0\n" \
        "v_mfma_f32_32x32x8_bf16 v[64:79], v[116:117], %4, v[64:79]\n" \
        VRD("ds_read_b128 v[118:121], %9\n") \
        "s_nop 7\n s_nop 7\n" \
        "1:\n" \
        INLOOP_DMA \
        "s_movk_i32 s40, 2048\n s_movk_i32 s41, 256\n s_movk_i32 s42, 2304\n" \
        "s_cmp_lg_u32 %10, 9\n s_cbranch_scc1 2f\n" \
        MIDBAR \
        "s_mov_b32 s40, 59392\n s_mov_b32 s41, 71936\n s_mov_b32 s42, 57600\n" \
        "2:\n" \
        KEYTILE("", 1024, "", 128, 576, "", 1152, PL, PH) \
        KEYTILE("v_add_u32 %7, s40, %7\n", 0, "v_add_u32 %8, s41, %8\n", 0, 1728, "v_add_u32 %9, s42, %9\n", 0, PL, PH) \
        "s_sub_u32 %10, %10, 1\n s_cmp_lg_u32 %10, 0\n s_cbranch_scc1 1b\n" \
        "s_waitcnt vmcnt(0) lgkmcnt(0)\n" \
        "v_smfmac_f32_32x32x32_bf16 %1, v[122:125], v[104:111], %6\n" \
        "s_setprio 0\n s_nop 7\n s_nop 7\n" \
        : "+v"(oA), "+v"(oB), "+v"(Q.fA), "+v"(Q.fB), "+v"(Q.rA), "+v"(Q.rB), "+v"(idx), "+v"(ka), "+v"(k2a), "+v"(va), "+s"(cnt), "+s"(pnext), "=&v"(zf) \
        : "s"(vmask), "v"(lane16), "s"(ldsb), "s"(src2), "v"(zoff), "s"(rs), "v"(vini) \
        : "v64", "v65", "v66", "v67", "v68", "v69", "v70", "v71", "v72", "v73", "v74", "v75", "v76", "v77", "v78", "v79", \
          "v80", "v81", "v82", "v83", "v84", "v85", "v86", "v87", "v88", "v89", "v90", "v91", "v92", "v93", "v94", "v95", \
          "v96", "v97", "v98", "v99", "v100", "v101", "v102", "v103", "v104", "v105", "v106", "v107", "v108", "v109", "v110", "v111", \
          "v112", "v113", "v114", "v115", "v116", "v117", "v118", "v119", "v120", "v121", "v122", "v123", "v124", "v125", \
          "v126", "s40", "s41", "s42", "s44", "scc", "memory")
    if (w < 4) { LOOPASM("s_setprio 0\n", "s_setprio 2\n", "s_setprio 2\n"); } else { LOOPASM("", "", "s_setprio 1\n"); }
#undef LOOPASM
#undef KEYTILE
#undef MIDBAR
#undef INLOOP_DMA
#undef DMA2
#undef VRD
#undef EXP4
}
struct AttnM { float a, b; };
__device__ __forceinline__ void attn_check(AttnQ& Q, f32x16& oA, f32x16& oB, AttnM& M, unsigned lds0, int w, int lane) {
    const int kpart = w >> 2;
    M.a = 0.f; M.b = 0.f;
    const bool okA = attn_range_ok(oA), okB = attn_range_ok(oB);
    if (!(okA && okB)) {
#pragma unroll 1
        for (int i = 0; i < 2; ++i) {
            if (i ? okB : okA) continue;
            f32x16 o; float mr;
            attn_slow(lds0 + kpart * CH_B, lane, i ? Q.fB : Q.fA, i ? Q.rB : Q.rA, o, mr);
            if (i) { oB = o; M.b = mr; } else { oA = o; M.a = mr; }
        }
    }
}
__device__ __forceinline__ void attn_give(const f32x16& oA, const f32x16& oB, const AttnM& M, unsigned lds0, int w, int lane) {
    const int kpart = w >> 2;
    LAS float* xch = (LAS float*)(uintptr_t)(lds0 + w * 3072);
#pragma unroll
    for (int r = 0; r < 11; ++r) xch[r * 64 + lane] = kpart ? oA[r] : oB[r];
    xch[11 * 64 + lane] = kpart ? M.a : M.b;
}
__device__ __forceinline__ void attn_store(const f32x16& oA, const f32x16& oB, const AttnM& M, rsrc_t rs, int head, int qtA, unsigned lds0, int w, int lane) {
    const int kpart = w >> 2, m = lane & 31, h = lane >> 5;
    LAS const float* xin = (LAS const float*)(uintptr_t)(lds0 + (w ^ 4) * 3072);
    const float m0 = kpart ? M.b : M.a, m1 = xin[11 * 64 + lane];
    const float mm = fmaxf(m0, m1), f0 = __builtin_amdgcn_exp2f(m0 - mm), f1 = __builtin_amdgcn_exp2f(m1 - mm);
    float v[11];
#pragma unroll
    for (int r = 0; r < 11; ++r) v[r] = (kpart ? oB[r] : oA[r]) * f0 + xin[r * 64 + lane] * f1;
    const float inv = 1.0f / v[10];
    const int tb = qtA + kpart;
    const u32x4 o8 = {cvtpk(v[0] * inv, v[1] * inv), cvtpk(v[2] * inv, v[3] * inv), cvtpk(v[4] * inv, v[5] * inv), cvtpk(v[6] * inv, v[7] * inv)};
    st16(rs, (unsigned)WS_OM + ((2 * tb + (m >> 4)) * 16 + (head >> 1)) * 1024 + ((2 * (head & 1) + h) * 16 + (m & 15)) * 16, o8);
    const unsigned rr = cvtpk(v[8] * inv, v[9] * inv);
    const unsigned r0 = __shfl(rr, (lane & 7) * 4), r1 = __shfl(rr, (lane & 7) * 4 + 1), r2 = __shfl(rr, (lane & 7) * 4 + 2), r3 = __shfl(rr, (lane & 7) * 4 + 3);
    st16(rs, lane < 8 ? (unsigned)WS_OR + (tb * 32 + head) * 128 + lane * 16 : OOB, (u32x4){r0, r1, r2, r3});
}

constexpr int P4_SLAB = 24576, P4_ZT = P4_SLAB + 576 * 144, P4_RED = P4_ZT + 72 * 144;
__device__ __forceinline__ void proj_dma(const u16* __restrict__ wo, int cg, int rot, unsigned lds0, int w, int lane) {
#pragma unroll
    for (int i = 0; i < 11; ++i) {
        int p = w + 8 * i + rot; p = p >= 81 ? p - 81 : p; p = p >= 81 ? p - 81 : p;
        const int ck = p * 64 + lane, row = ck / 9, c9 = ck - row * 9;
        glds16(wo + (size_t)row * D + cg * 72 + c9 * 8, lds0 + P4_SLAB + p * 1024);
    }
}
struct GateRegs { u32x4 z0, z1, q0, q1; };
__device__ __forceinline__ void gate_issue(GateRegs& R, rsrc_t rs, const u16* __restrict__ bq, int tb, int cg, int w, int lane) {
    const int tid = threadIdx.x;
    R.z0 = ld16(rs, (unsigned)WS_ZT + (unsigned)(cg * 72 + (tid >> 3)) * (S * 2) + (tb * 64 + (tid & 7) * 8) * 2);
    R.z1 = R.z0;
    if (tid < 64) R.z1 = ld16(rs, (unsigned)WS_ZT + (unsigned)(cg * 72 + 64 + (tid >> 3)) * (S * 2) + (tb * 64 + (tid & 7) * 8) * 2);
    const int G = lane >> 4;
    { const int u = 4 * w + G, tg = u / 9, cb = u - tg * 9; R.q0 = *(const u32x4*)(bq + (size_t)(tb * 64 + tg * 16 + (lane & 15)) * D + cg * 72 + cb * 8); }
    R.q1 = R.q0;
    if (w == 0) { const int u = 32 + G, tg = u / 9, cb = u - tg * 9; R.q1 = *(const u32x4*)(bq + (size_t)(tb * 64 + tg * 16 + (lane & 15)) * D + cg * 72 + cb * 8); }
}
__device__ __forceinline__ void gate_stage(const GateRegs& R, unsigned lds0) {
    const int tid = threadIdx.x;
    ldswr4(lds0 + P4_ZT + (tid >> 3) * 144 + (tid & 7) * 16, R.z0);
    if (tid < 64) ldswr4(lds0 + P4_ZT + (64 + (tid >> 3)) * 144 + (tid & 7) * 16, R.z1);
}
__device__ __forceinline__ void gate_unit(unsigned lds0, u16* __restrict__ out, const u32x4& qq, int u, int tb, int cg, int lane) {
    const int tg = u / 9, cb = u - tg * 9, q = (lane & 15) >> 2, p = lane & 3;
    const unsigned a = lds0 + P4_ZT + (8 * cb + q) * 144 + (16 * tg + 4 * p) * 2;
    const u32x2 zl = __builtin_bit_cast(u32x2, trread(a)), zh = __builtin_bit_cast(u32x2, trread(a + 4 * 144));
    const u32x4 r = {cvtpk(bflo(qq[0]) * bflo(zl[0]), bfhi(qq[0]) * bfhi(zl[0])), cvtpk(bflo(qq[1]) * bflo(zl[1]), bfhi(qq[1]) * bfhi(zl[1])),
                     cvtpk(bflo(qq[2]) * bflo(zh[0]), bfhi(qq[2]) * bfhi(zh[0])), cvtpk(bflo(qq[3]) * bflo(zh[1]), bfhi(qq[3]) * bfhi(zh[1]))};
    *(u32x4*)(out + (size_t)(tb * 64 + tg * 16 + (lane & 15)) * OUTW + D + cg * 72 + cb * 8) = r;
}
__device__ __forceinline__ void gate_finish(const GateRegs& R, unsigned lds0, u16* out, int tb, int cg, int w, int lane) {
    gate_unit(lds0, out, R.q0, 4 * w + (lane >> 4), tb, cg, lane);
    if (w == 0) gate_unit(lds0, out, R.q1, 32 + (lane >> 4), tb, cg, lane);
}
struct ProjW { bf16x8 f[9][5]; };
__device__ __forceinline__ void proj_readw(ProjW& W, unsigned lds0, int w, int lane) {
    const int kh = w >> 2, kq = lane >> 4, q = (lane & 15) >> 2, p = lane & 3;
#pragma unroll
    for (int s = 0; s < 9; ++s) {
        const int st = kh * 9 + s;
        int r0, r1;
        if (st < 16) { const int hd = 2 * st + (kq >> 1), hh = kq & 1; r0 = 18 * hd + 4 * hh + q; r1 = r0 + 8; }
        else { const int hd = 16 * (st - 16) + 4 * kq + (q >> 1); r0 = 18 * hd + 16 + (q & 1); r1 = r0 + 36; }
#pragma unroll
        for (int c = 0; c < 5; ++c) {
            const int cc = (c == 4 && p >= 2) ? 64 + (p & 1) * 4 : 16 * c + 4 * p;
            W.f[s][c] = cat8(trread(lds0 + P4_SLAB + r0 * 144 + cc * 2), trread(lds0 + P4_SLAB + r1 * 144 + cc * 2));
        }
    }
}
__device__ __forceinline__ void proj_tile(const ProjW& W, rsrc_t rs, u16* __restrict__ out, int tb64, int cg, unsigned lds0, int w, int lane) {
    const int tt = w & 3, kh = w >> 2, t16 = tb64 * 4 + tt, kq = lane >> 4;
    bf16x8 af[9];
#pragma unroll
    for (int s = 0; s < 9; ++s) {
        const int st = kh * 9 + s;
        if (st < 16) af[s] = __builtin_bit_cast(bf16x8, ld16(rs, (unsigned)WS_OM + (t16 * 16 + st) * 1024 + lane * 16));
        else {
            const unsigned ro = (unsigned)WS_OR + ((t16 >> 1) * 32 + 16 * (st - 16) + 4 * (lane >> 4)) * 128 + ((t16 & 1) * 16 + (lane & 15)) * 4;
            af[s] = __builtin_bit_cast(bf16x8, (u32x4){ld4(rs, ro), ld4(rs, ro + 128), ld4(rs, ro + 256), ld4(rs, ro + 384)});
        }
    }
    f32x4 acc[5] = {};
#pragma unroll
    for (int s = 0; s < 9; ++s)
#pragma unroll
        for (int c = 0; c < 5; ++c) acc[c] = MFMA16(W.f[s][c], af[s], acc[c]);
    LAS f32x4* red = (LAS f32x4*)(uintptr_t)(lds0 + P4_RED + tt * 5120);
    if (kh == 1) {
#pragma unroll
        for (int c = 0; c < 5; ++c) red[c * 64 + lane] = acc[c];
    }
    LBAR();
    if (kh == 0) {
        u16* op = out + (size_t)(t16 * 16 + (lane & 15)) * OUTW + cg * 72 + 4 * kq;
#pragma unroll
        for (int c = 0; c < 5; ++c) {
            const f32x4 v = acc[c] + red[c * 64 + lane];
            if (c < 4 || kq < 2) *(u32x2*)(op + 16 * c) = (u32x2){cvtpk(v[0], v[1]), cvtpk(v[2], v[3])};
        }
    }
}

__global__ __launch_bounds__(512, 2) void fused(const u16* __restrict__ x, const u16* __restrict__ wqkv, const u16* __restrict__ wo,
                                                const u16* __restrict__ bq, const u16* __restrict__ bk, const u16* __restrict__ bv,
                                                const u16* __restrict__ shift_k, const u16* __restrict__ ssm_k, const u16* __restrict__ ssm_D,
                                                char* __restrict__ ws, u16* __restrict__ out) {
    extern __shared__ __attribute__((aligned(16))) char lds[];
    const unsigned lds0 = (unsigned)(uintptr_t)lds;
    const int lane = threadIdx.x & 63, w = __builtin_amdgcn_readfirstlane(threadIdx.x >> 6), b = blockIdx.x;
    const rsrc_t rs = ws_rsrc(ws);
    STAMP_DECL;
    STAMP(0, 0);
    const unsigned epoch = *(const __attribute__((address_space(4))) unsigned*)(uintptr_t)ws + 1u;
    if (threadIdx.x == 0) ldswr1(lds0 + LDS_CNT, 0u);
    const bool gemm = b < 224;
    ConvRegs CR;
    const int mb = ((b & 7) >> 1) * 4 + ((b >> 3) & 3), ng = (b & 1) * 7 + (b >> 5);
    if (gemm) {
        qkv_tile(ng >= 9, x, wqkv, rs, mb, ng, 0, lds0, w, lane, bk, bv, shift_k, b & 31, b >> 5, epoch, CR, ssm_k, b STAMP_PASS);
    } else kv_only(bk, bv, shift_k, rs, lds0, w, lane, epoch);
    STAMP(0, 2);
    const int head = (b & 7) * 4 + ((b >> 3) & 3), qb = b >> 5, qtA = qb * 8 + (w & 3) * 2;
    AttnQ Q;
    const int rounds = gemm ? 1 : 2, pair0 = gemm ? b : 224 + 2 * (b - 224);
#pragma unroll 1
    for (int rd = 0; rd < rounds; ++rd) {
        const int pair = pair0 + rd, c = 2 * pair + (w >> 2);
        const bool last = rd + 1 == rounds;
        const unsigned base = lds0 + CV_BASE + (w >> 2) * CV_B;
        if (!gemm) {
            if (w == 0) poll_flags(rs, FL_KV + (pair >> 5) * 128, 32, epoch, lane);
            LBAR();
            conv_issue(CR, ssm_k, rs, c, w);
        }
        conv_build(CR, base);
        WAITBAR(0);
        if (gemm && w == 0) publish_qkv(rs, mb, ng, epoch, lane);
        STAMP(1, 3);
        f32x16 d0, d1;
        conv_main(base, lds0, w, lane, d0, d1, last, head, rs, epoch STAMP_PASS);
        STAMP(1, 4);
        WAITBAR(0);
        STAMP(1, 5);
        if (last) { attn_loadq(Q, ws, head, qtA, lane); attn_dma<0, 3>(ws, head, 9 * qb, lds0, w, lane); }
        conv_partials(base, w & 3, lane, d0, d1);
        if (last) attn_dma<3, 6>(ws, head, 9 * qb, lds0, w, lane);
        LBAR();
        STAMP(1, 6);
        ConvOut co;
        conv_sum(co, base, ssm_D, c, CR.u);
        if (last) attn_dma<6, 9>(ws, head, 9 * qb, lds0, w, lane);
        st16(rs, (unsigned)WS_ZT + (unsigned)c * (S * 2) + (threadIdx.x & 255) * 16, co.z);
    }
    STAMP(1, 7);
    ATTN_QWAITBAR(Q, 0);
    publish(rs, FL_Z + b * 4, epoch);
    attn_fixq(Q, lane);
    STAMP(1, 8);
    f32x16 oA, oB;
    u32x4 zf;
    attn_loop(Q, ws, rs, head, 9 * qb, lds0, w, lane, oA, oB, zf);
    STAMP(1, 9);
    if (!__all(zf[0] == epoch && zf[1] == epoch && zf[2] == epoch && zf[3] == epoch)) poll_flags(rs, FL_Z, 256, epoch, lane);
    GateRegs G;
    gate_issue(G, rs, bq, b >> 3, b & 7, w, lane);
    AttnM M;
    attn_check(Q, oA, oB, M, lds0, w, lane);
    LBAR();
    attn_give(oA, oB, M, lds0, w, lane);
    LBAR();
    attn_store(oA, oB, M, rs, head, qtA, lds0, w, lane);
    gate_stage(G, lds0);
    STAMP(1, 10);
    const int i5 = b & 31, ptb = (b >> 5) * 4 + ((i5 & 7) >> 1), pcg = (i5 & 1) * 4 + (i5 >> 3);
    proj_dma(wo, pcg, 10 * (b >> 5), lds0, w, lane);
    WAITBAR(11);
    if (w == 0) st4(rs, lane < 32 ? FL_O + ((b & ~31) + lane) * 128 + (b & 31) * 4 : 0xFFFFFF00u, epoch);
    if (b == 0 && threadIdx.x == 0) *(unsigned*)ws = epoch;
    STAMP(1, 11);
    gate_finish(G, lds0, out, b >> 3, b & 7, w, lane);
    WAITBAR(1);
    ProjW W;
    proj_readw(W, lds0, w, lane);
    if (w == 0) poll_at(rs, FL_O + b * 128 + lane * 16, lane < 8, epoch);
    LBAR();
    STAMP(1, 13);
    proj_tile(W, rs, out, ptb, pcg, lds0, w, lane);
    STAMP(1, 14);
    STAMP_FLUSH(0);
}

extern "C" void kernel_launch(void* const* d_in, const int* in_sizes, int n_in, void* d_out, int out_size, void* d_ws, size_t ws_size,
                              hipStream_t stream) {
    (void)hipFuncSetAttribute((const void*)fused, hipFuncAttributeMaxDynamicSharedMemorySize, LDS_BYTES);
    fused<<<256, 512, LDS_BYTES, stream>>>((const u16*)d_in[0], (const u16*)d_in[1], (const u16*)d_in[2], (const u16*)d_in[3], (const u16*)d_in[4],
                                           (const u16*)d_in[5], (const u16*)d_in[6], (const u16*)d_in[7], (const u16*)d_in[8], (char*)d_ws, (u16*)d_out);
}
```
